```python
import torch

def _softmax(x, axis=-1):
    return torch.softmax(x, dim=axis)
def _gelu(x):
    return torch.nn.functional.gelu(x, approximate="tanh")
def _silu(x):
    return torch.nn.functional.silu(x)
def _sigmoid(x):
    return torch.sigmoid(x)
def _layernorm(x, axis=-1, eps=1e-5):
    m = x.mean(dim=axis, keepdim=True)
    v = x.var(dim=axis, keepdim=True, unbiased=False)
    return (x - m) / torch.sqrt(v + eps)
def _scatter_add(tgt, idx, src, axis=0):
    return tgt.clone().index_add_(axis, idx, src)
def _segment_sum(src, seg, n):
    out = torch.zeros((n,) + src.shape[1:], dtype=src.dtype)
    return out.index_add_(0, seg, src)
def _pad(x, pw):
    flat = tuple(v for pair in reversed(pw) for v in pair)
    return torch.nn.functional.pad(x, flat)

kernel_name = 'sequential_preLN__gqa_block_cached__geglu_ffn__decode'

def setup_inputs():

    _w = (lambda a: (lambda t: t.to(torch.bfloat16) if t.is_floating_point() else t)(torch.as_tensor(a)))
    def _A_setup():
        B, Sq, D = 2, 1, 448
        Hq, Hkv, Sk, Dh = 16, 8, 32768, 256
        assert Hq % Hkv == 0, (Hq, Hkv)
        return {
            "x": _w((lambda *s: torch.randn(*s, dtype=torch.bfloat16) * 0.05)(B, Sq, D)),
            "k_cache": _w((lambda *s: torch.randn(*s, dtype=torch.bfloat16) * 3.2)(B, Hkv, Sk, Dh)),
            "v_cache": _w((lambda *s: torch.randn(*s, dtype=torch.bfloat16) * 0.4)(B, Hkv, Sk, Dh)),
            "wq": _w((lambda *s: torch.randn(*s, dtype=torch.bfloat16) * 3.2)(D, Hq * Dh) * (D ** -0.5)),
            "wo": _w((lambda *s: torch.randn(*s, dtype=torch.bfloat16) * 0.4)(Hq * Dh, D) * ((Hq * Dh) ** -0.5)),
        }
    _A_in = _A_setup()

    def _B_setup():
        B, S, D, F = 2, 1, 448, 1792
        return {
            "x": _w((lambda *s: torch.randn(*s, dtype=torch.bfloat16) * 0.05)(B, S, D)),
            "w_gate": _w((lambda *s: torch.randn(*s, dtype=torch.bfloat16) * 0.05)(D, F)),
            "w_up": _w((lambda *s: torch.randn(*s, dtype=torch.bfloat16) * 0.05)(D, F)),
            "w_down": _w((lambda *s: torch.randn(*s, dtype=torch.bfloat16) * 0.05)(F, D)),
        }
    _B_in = _B_setup()

    return {
        "A_x": _A_in["x"],
        "A_k_cache": _A_in["k_cache"],
        "A_v_cache": _A_in["v_cache"],
        "A_wq": _A_in["wq"],
        "A_wo": _A_in["wo"],
        "B_x": _B_in["x"],
        "B_w_gate": _B_in["w_gate"],
        "B_w_up": _B_in["w_up"],
        "B_w_down": _B_in["w_down"],
    }

def reference(A_x, A_k_cache, A_v_cache, A_wq, A_wo, B_x, B_w_gate, B_w_up, B_w_down):
    def _A_ref(x, k_cache, v_cache, wq, wo):
        B, Sq, D = x.shape
        Hq, Dh = 16, 256
        Hkv = k_cache.shape[1]
        rep = Hq // Hkv
        q = (lambda x, *d: x.permute(*d))((x @ wq).reshape(B, Sq, Hq, Dh), 0, 2, 1, 3)
        k = (lambda x, n, axis: torch.repeat_interleave(x, n, dim=axis))(k_cache, rep, axis=1)
        v = (lambda x, n, axis: torch.repeat_interleave(x, n, dim=axis))(v_cache, rep, axis=1)
        scores = torch.einsum("bhqd,bhkd->bhqk", q, k) * (Dh ** -0.5)
        p = _softmax(scores, axis=-1)
        o = torch.einsum("bhqk,bhkd->bhqd", p, v)
        return (lambda x, *d: x.permute(*d))(o, 0, 2, 1, 3).reshape(B, Sq, Hq * Dh) @ wo

    def _B_ref(x, w_gate, w_up, w_down):
        return (_gelu(x @ w_gate) * (x @ w_up)) @ w_down

    x = A_x
    h = x + _A_ref(_layernorm(x, axis=-1), A_k_cache, A_v_cache, A_wq, A_wo)
    h2 = h + _B_ref(_layernorm(h, axis=-1), B_w_gate, B_w_up, B_w_down)
    return h2
```

```cpp
#include <hip/hip_runtime.h>
#include <hip/hip_bf16.h>
#include <stdint.h>

#define MIMIC 0
typedef unsigned short u16;
typedef unsigned u32x4 __attribute__((ext_vector_type(4)));
typedef float f32x4 __attribute__((ext_vector_type(4)));
typedef __bf16 bf16x2 __attribute__((ext_vector_type(2)));

constexpr int B = 2, D = 448, HQ = 16, HKV = 8, SK = 32768, DH = 256, F = 1792;
constexpr int NSPLIT = 32;
constexpr int KEYS_PER_WG = SK / NSPLIT;

__device__ __forceinline__ float bf2f(u16 v) { return __uint_as_float((unsigned)v << 16); }
__device__ __forceinline__ u16 f2bf(float f) { __bf16 b = (__bf16)f; return __builtin_bit_cast(u16, b); }
__device__ __forceinline__ float rbf(float f) { return bf2f(f2bf(f)); }
__device__ __forceinline__ float mim(float f) { return MIMIC ? rbf(f) : f; }
__device__ __forceinline__ float dot2(unsigned a, unsigned b, float c) {
  return __builtin_amdgcn_fdot2_f32_bf16(__builtin_bit_cast(bf16x2, a), __builtin_bit_cast(bf16x2, b), c, false);
}
__device__ __forceinline__ float wave_sum(float v) {
  for (int o = 32; o > 0; o >>= 1) v += __shfl_xor(v, o);
  return v;
}

__device__ void ln_row(const float* src, float* dst, int lane) {
  float v[7]; float s = 0.f;
  for (int i = 0; i < 7; ++i) { v[i] = src[lane + 64 * i]; s += v[i]; }
  s = wave_sum(s);
  float mu = s * (1.f / D), ss = 0.f;
  for (int i = 0; i < 7; ++i) { float d = v[i] - mu; ss += d * d; }
  ss = wave_sum(ss);
  float var = mim(ss * (1.f / D));
  float mub = mim(mu);
  float den = mim(sqrtf(mim(var + 1e-5f)));
  for (int i = 0; i < 7; ++i) dst[lane + 64 * i] = mim(mim(v[i] - mub) / den);
}

__global__ void __launch_bounds__(256) k_q(const u16* x, const u16* wq, u16* q) {
  __shared__ float xs[2][D], xn[2][D], red[4][2][64];
  int tid = threadIdx.x, lane = tid & 63, w = tid >> 6;
  for (int i = tid; i < 2 * D; i += 256) xs[i / D][i % D] = bf2f(x[i]);
  __syncthreads();
  if (w < 2) ln_row(xs[w], xn[w], lane);
  __syncthreads();
  int col = blockIdx.x * 64 + lane;
  float a0 = 0.f, a1 = 0.f;
  for (int r = w; r < D; r += 4) { float wv = bf2f(wq[(size_t)r * 4096 + col]); a0 += xn[0][r] * wv; a1 += xn[1][r] * wv; }
  red[w][0][lane] = a0; red[w][1][lane] = a1;
  __syncthreads();
  if (w < 2) { float s = red[0][w][lane] + red[1][w][lane] + red[2][w][lane] + red[3][w][lane]; q[w * 4096 + col] = f2bf(s); }
}

template <int CTRL> __device__ __forceinline__ float dpp(float x) {
  return __builtin_bit_cast(float, __builtin_amdgcn_mov_dpp(__builtin_bit_cast(int, x), CTRL, 0xf, 0xf, true));
}
constexpr int XOR1 = 0xB1, XOR2 = 0x4E, XOR7 = 0x141, XOR8 = 0x128;

__global__ void __launch_bounds__(256) k_attn(const u16* q, const u16* K, const u16* V, float* pml, float* po) {
  __shared__ float lds_p[4][2][16];
  __shared__ float mrg[4][2][4 + DH];
  int s = blockIdx.x % NSPLIT, bh = blockIdx.x / NSPLIT, b = bh / HKV, kvh = bh % HKV;
  int tid = threadIdx.x, lane = tid & 63, w = __builtin_amdgcn_readfirstlane(tid >> 6), r = lane >> 5, c = lane & 31;
  u32x4 q0 = *(const u32x4*)(q + b * 4096 + (2 * kvh) * DH + 8 * c);
  u32x4 q1 = *(const u32x4*)(q + b * 4096 + (2 * kvh + 1) * DH + 8 * c);
  const u16* kb = K + ((size_t)bh * SK + (size_t)s * KEYS_PER_WG) * DH + lane * 8;
  const u16* vb = V + ((size_t)bh * SK + (size_t)s * KEYS_PER_WG) * DH + lane * 8;
  constexpr int NT = KEYS_PER_WG / 16 / 4;
  float m = -INFINITY, l = 0.f, acc0[8], acc1[8];
  for (int i = 0; i < 8; ++i) acc0[i] = acc1[i] = 0.f;
  u32x4 Kn[8], Vn[8];
#pragma unroll
  for (int t = 0; t < 8; ++t) { Kn[t] = __builtin_nontemporal_load((const u32x4*)(kb + (size_t)(w * 16 + 2 * t) * DH)); }
#pragma unroll
  for (int t = 0; t < 8; ++t) { Vn[t] = __builtin_nontemporal_load((const u32x4*)(vb + (size_t)(w * 16 + 2 * t) * DH)); }
  for (int it = 0; it < NT; ++it) {
    u32x4 Kc[8], Vc[8];
#pragma unroll
    for (int t = 0; t < 8; ++t) { Kc[t] = Kn[t]; Vc[t] = Vn[t]; }
    if (it + 1 < NT) {
      size_t o = (size_t)((it + 1) * 64 + w * 16) * DH;
#pragma unroll
      for (int t = 0; t < 8; ++t) Kn[t] = __builtin_nontemporal_load((const u32x4*)(kb + o + (size_t)(2 * t) * DH));
#pragma unroll
      for (int t = 0; t < 8; ++t) Vn[t] = __builtin_nontemporal_load((const u32x4*)(vb + o + (size_t)(2 * t) * DH));
    }
    float x[16];
#pragma unroll
    for (int t = 0; t < 8; ++t) {
      float d0 = 0.f, d1 = 0.f;
#pragma unroll
      for (int k = 0; k < 4; ++k) { d0 = dot2(Kc[t][k], q0[k], d0); d1 = dot2(Kc[t][k], q1[k], d1); }
      x[2 * t] = d0; x[2 * t + 1] = d1;
    }
#define TR(HALF, CTRL, BIT) _Pragma("unroll") for (int h = 0; h < HALF; ++h) { float lo = x[h], hi = x[h + HALF]; float t1 = lo + dpp<CTRL>(lo), t2 = hi + dpp<CTRL>(hi); x[h] = (c & BIT) ? t2 : t1; }
    TR(8, XOR8, 8) TR(4, XOR7, 4) TR(2, XOR2, 2) TR(1, XOR1, 1)
    float sc = x[0];
    sc += __shfl_xor(sc, 16);
    sc = mim(sc) * (0.0625f * 1.4426950408889634f);
    float pm = sc;
    pm = fmaxf(pm, __shfl_xor(pm, 2)); pm = fmaxf(pm, __shfl_xor(pm, 4)); pm = fmaxf(pm, __shfl_xor(pm, 8)); pm = fmaxf(pm, __shfl_xor(pm, 32));
    float mn = fmaxf(m, pm);
    float alpha = __builtin_amdgcn_exp2f(m - mn), p = __builtin_amdgcn_exp2f(sc - mn);
    l = l * alpha + p; m = mn;
    float a0 = __shfl(alpha, 0), a1 = __shfl(alpha, 1);
#pragma unroll
    for (int i = 0; i < 8; ++i) { acc0[i] *= a0; acc1[i] *= a1; }
    if (c < 16) lds_p[w][r][c] = p;
    __builtin_amdgcn_s_waitcnt(0xC07F);
    __builtin_amdgcn_wave_barrier();
    f32x4 pr[4];
#pragma unroll
    for (int u = 0; u < 4; ++u) pr[u] = *(const f32x4*)&lds_p[w][r][4 * u];
#pragma unroll
    for (int t = 0; t < 8; ++t) {
      float p0 = pr[(2 * t) >> 2][(2 * t) & 3], p1 = pr[(2 * t + 1) >> 2][(2 * t + 1) & 3];
#pragma unroll
      for (int k = 0; k < 4; ++k) {
        float lo = __uint_as_float(Vc[t][k] << 16), hi = __uint_as_float(Vc[t][k] & 0xffff0000u);
        acc0[2 * k] = fmaf(p0, lo, acc0[2 * k]); acc0[2 * k + 1] = fmaf(p0, hi, acc0[2 * k + 1]);
        acc1[2 * k] = fmaf(p1, lo, acc1[2 * k]); acc1[2 * k + 1] = fmaf(p1, hi, acc1[2 * k + 1]);
      }
    }
    __builtin_amdgcn_wave_barrier();
  }
  for (int i = 0; i < 8; ++i) { acc0[i] += __shfl_xor(acc0[i], 32); acc1[i] += __shfl_xor(acc1[i], 32); }
  l += __shfl_xor(l, 2); l += __shfl_xor(l, 4); l += __shfl_xor(l, 8); l += __shfl_xor(l, 32);
  if (r == 0) { for (int i = 0; i < 8; ++i) { mrg[w][0][4 + 8 * c + i] = acc0[i]; mrg[w][1][4 + 8 * c + i] = acc1[i]; } }
  if (lane < 2) { mrg[w][lane][0] = m; mrg[w][lane][1] = l; }
  __syncthreads();
  {
    int h = tid >> 7, d0 = (tid & 127) * 2;
    float M = fmaxf(fmaxf(mrg[0][h][0], mrg[1][h][0]), fmaxf(mrg[2][h][0], mrg[3][h][0]));
    float L = 0.f, o0 = 0.f, o1 = 0.f;
    for (int ww = 0; ww < 4; ++ww) { float wt = __builtin_amdgcn_exp2f(mrg[ww][h][0] - M); L += wt * mrg[ww][h][1]; o0 += wt * mrg[ww][h][4 + d0]; o1 += wt * mrg[ww][h][4 + d0 + 1]; }
    size_t slot = (size_t)(b * HQ + 2 * kvh + h) * NSPLIT + s;
    po[slot * DH + d0] = o0; po[slot * DH + d0 + 1] = o1;
    if ((tid & 127) == 0) { pml[slot * 2] = M; pml[slot * 2 + 1] = L; }
  }
}

__global__ void __launch_bounds__(256) k_comb(const float* pml, const float* po, float* o) {
  int row = blockIdx.x, d = threadIdx.x;
  float M = -INFINITY;
  for (int s = 0; s < NSPLIT; ++s) M = fmaxf(M, pml[(row * NSPLIT + s) * 2]);
  float L = 0.f, a = 0.f;
  for (int s = 0; s < NSPLIT; ++s) { float wt = __builtin_amdgcn_exp2f(pml[(row * NSPLIT + s) * 2] - M); L += wt * pml[(row * NSPLIT + s) * 2 + 1]; a += wt * po[(size_t)(row * NSPLIT + s) * DH + d]; }
  o[row * DH + d] = mim(a / L);
}

__global__ void __launch_bounds__(256) k_wo(const float* o, const u16* wo, const u16* x, float* h) {
  __shared__ float red[256][16];
  int tid = threadIdx.x, c0 = blockIdx.x * 8;
  float a[16]; for (int i = 0; i < 16; ++i) a[i] = 0.f;
  for (int r = tid; r < 4096; r += 256) {
    u32x4 wv = *(const u32x4*)(wo + (size_t)r * D + c0);
    float o0 = o[r], o1 = o[4096 + r];
    for (int k = 0; k < 4; ++k) { float lo = __uint_as_float(wv[k] << 16), hi = __uint_as_float(wv[k] & 0xffff0000u);
      a[2 * k] += o0 * lo; a[2 * k + 1] += o0 * hi; a[8 + 2 * k] += o1 * lo; a[8 + 2 * k + 1] += o1 * hi; }
  }
  for (int i = 0; i < 16; ++i) red[tid][i] = a[i];
  __syncthreads();
  if (tid < 16) { float s = 0.f; for (int t = 0; t < 256; ++t) s += red[t][tid]; int b = tid >> 3, c = c0 + (tid & 7); h[b * D + c] = mim(bf2f(x[b * D + c]) + mim(s)); }
}

__global__ void __launch_bounds__(256) k_gu(const float* h, const u16* wg, const u16* wu, float* g) {
  __shared__ float hs[2][D], xn[2][D], red[4][4][64];
  int tid = threadIdx.x, lane = tid & 63, w = tid >> 6;
  for (int i = tid; i < 2 * D; i += 256) hs[i / D][i % D] = h[i];
  __syncthreads();
  if (w < 2) ln_row(hs[w], xn[w], lane);
  __syncthreads();
  int col = blockIdx.x * 64 + lane;
  float a0 = 0.f, a1 = 0.f, u0 = 0.f, u1 = 0.f;
  for (int r = w; r < D; r += 4) { float gv = bf2f(wg[(size_t)r * F + col]), uv = bf2f(wu[(size_t)r * F + col]); a0 += xn[0][r] * gv; a1 += xn[1][r] * gv; u0 += xn[0][r] * uv; u1 += xn[1][r] * uv; }
  red[w][0][lane] = a0; red[w][1][lane] = a1; red[w][2][lane] = u0; red[w][3][lane] = u1;
  __syncthreads();
  if (w < 2) {
    float a = mim(red[0][w][lane] + red[1][w][lane] + red[2][w][lane] + red[3][w][lane]);
    float u = mim(red[0][2 + w][lane] + red[1][2 + w][lane] + red[2][2 + w][lane] + red[3][2 + w][lane]);
    float t = tanhf(0.7978845608028654f * (a + 0.044715f * a * a * a));
    float ge = mim(0.5f * a * (1.f + t));
    g[w * F + col] = mim(ge * u);
  }
}

__global__ void __launch_bounds__(256) k_down(const float* g, const u16* wd, const float* h, u16* out) {
  __shared__ float red[256][16];
  int tid = threadIdx.x, c0 = blockIdx.x * 8;
  float a[16]; for (int i = 0; i < 16; ++i) a[i] = 0.f;
  for (int r = tid; r < F; r += 256) {
    u32x4 wv = *(const u32x4*)(wd + (size_t)r * D + c0);
    float o0 = g[r], o1 = g[F + r];
    for (int k = 0; k < 4; ++k) { float lo = __uint_as_float(wv[k] << 16), hi = __uint_as_float(wv[k] & 0xffff0000u);
      a[2 * k] += o0 * lo; a[2 * k + 1] += o0 * hi; a[8 + 2 * k] += o1 * lo; a[8 + 2 * k + 1] += o1 * hi; }
  }
  for (int i = 0; i < 16; ++i) red[tid][i] = a[i];
  __syncthreads();
  if (tid < 16) { float s = 0.f; for (int t = 0; t < 256; ++t) s += red[t][tid]; int b = tid >> 3, c = c0 + (tid & 7); out[b * D + c] = f2bf(h[b * D + c] + mim(s)); }
}

extern "C" void kernel_launch(void* const* d_in, const int* in_sizes, int n_in, void* d_out, int out_size, void* d_ws, size_t ws_size, hipStream_t stream) {
  const u16* x = (const u16*)d_in[0]; const u16* K = (const u16*)d_in[1]; const u16* V = (const u16*)d_in[2];
  const u16* wq = (const u16*)d_in[3]; const u16* wo = (const u16*)d_in[4];
  const u16* wg = (const u16*)d_in[6]; const u16* wu = (const u16*)d_in[7]; const u16* wd = (const u16*)d_in[8];
  char* ws = (char*)d_ws;
  u16* q = (u16*)ws; float* pml = (float*)(ws + 65536); float* po = (float*)(ws + 131072);
  float* o = (float*)(ws + (8 << 20)); float* h = (float*)(ws + (9 << 20)); float* g = (float*)(ws + (10 << 20));
  k_q<<<64, 256, 0, stream>>>(x, wq, q);
  k_attn<<<B * HKV * NSPLIT, 256, 0, stream>>>(q, K, V, pml, po);
  k_comb<<<B * HQ, 256, 0, stream>>>(pml, po, o);
  k_wo<<<56, 256, 0, stream>>>(o, wo, x, h);
  k_gu<<<28, 256, 0, stream>>>(h, wg, wu, g);
  k_down<<<56, 256, 0, stream>>>(g, wd, h, (u16*)d_out);
}
```

```cpp
#include <hip/hip_runtime.h>
#include <hip/hip_bf16.h>
#include <stdint.h>

#define LAS __attribute__((address_space(3)))
typedef unsigned short u16;
typedef unsigned long long u64;
typedef unsigned u32x2 __attribute__((ext_vector_type(2)));
typedef unsigned u32x4 __attribute__((ext_vector_type(4)));
typedef float f32x2 __attribute__((ext_vector_type(2)));
typedef float f32x4 __attribute__((ext_vector_type(4)));
typedef short s16x4 __attribute__((ext_vector_type(4)));
typedef __bf16 bf16x2 __attribute__((ext_vector_type(2)));
typedef __bf16 bf16x8 __attribute__((ext_vector_type(8)));
typedef __amdgpu_buffer_rsrc_t rsrc_t;

#define NFL 21
#define NFLW 40
#define AUX_W 2
#define AUX_Q 2
#define GM 0
#define RPRE 3

constexpr int DM = 448, SK = 32768, DH = 256, FF = 1792;
constexpr int NU = 128;
constexpr int NS = 16, KBLK = 1040, VBLK = 1056, SLOT = 8 * VBLK;
constexpr int L_STG = NS * SLOT, L_XN = L_STG + 14336, L_QB = L_XN + 1792, L_HB = L_QB + 1024, L_GB = L_HB + 3584,
              L_OB = L_GB + 128, L_RED = L_OB + 2048, L_CTL = L_RED + 64, L_STAMP = L_CTL + 256, L_END = L_STAMP + 1280;
constexpr int C_PROG = 0, C_LANDED = 4, C_XN = 5, C_Q = 6, C_VER = 7, C_MX = 8, C_P = 9;
constexpr int W_EPOCH = 0, W_FLAG = 4096, W_Q = 8192, W_ML = 40960, W_Y = 49152, W_PART = 131072, W_Z = 655360,
              W_END = 1 << 20;
constexpr int WO0 = 144, FFN0 = 200, NFFN = 56;
constexpr unsigned SENT = 0xFFFFFFFFu;
constexpr int AUX_SC1 = 16, AUX_NT = 2;
constexpr int SPIN_CAP = 1 << 20;
constexpr float LOG2E = 1.4426950408889634f, C2 = 0.0625f * LOG2E;

#define WAITV(n) asm volatile("s_waitcnt vmcnt(%0)" :: "i"(n) : "memory")
#define WAITL() asm volatile("s_waitcnt lgkmcnt(0)" ::: "memory")
#define CFENCE() asm volatile("" ::: "memory")
#define BARRIER() do { WAITL(); __builtin_amdgcn_s_barrier(); CFENCE(); } while (0)

#define STAMP(k) do { } while (0)
#define CYC(k) do { } while (0)
#define FLUSH() do { } while (0)

__device__ __forceinline__ float bf_lo(unsigned v) { return __uint_as_float(v << 16); }
__device__ __forceinline__ float bf_hi(unsigned v) { return __uint_as_float(v & 0xffff0000u); }
__device__ __forceinline__ unsigned pack2(float lo, float hi) { return __builtin_bit_cast(unsigned, __builtin_convertvector(f32x2{lo, hi}, bf16x2)); }
__device__ __forceinline__ float rbf(float f) { return bf_lo(pack2(f, f)); }
__device__ __forceinline__ unsigned pub(float f) { unsigned u = __float_as_uint(f); return u == SENT ? 0x7FC00000u : u; }
template <int CTRL> __device__ __forceinline__ float dpp(float x) {
  return __builtin_bit_cast(float, __builtin_amdgcn_mov_dpp(__builtin_bit_cast(int, x), CTRL, 0xf, 0xf, true));
}
constexpr int XOR1 = 0xB1, XOR2 = 0x4E, XOR7 = 0x141, ROR8 = 0x128, ROR4 = 0x124, NEWBC = 0x150;
__device__ __forceinline__ float xrow_max(float x) {
  auto s = __builtin_amdgcn_permlane16_swap(__float_as_uint(x), __float_as_uint(x), false, false);
  x = fmaxf(__uint_as_float(s[0]), __uint_as_float(s[1]));
  auto t = __builtin_amdgcn_permlane32_swap(__float_as_uint(x), __float_as_uint(x), false, false);
  return fmaxf(__uint_as_float(t[0]), __uint_as_float(t[1]));
}
__device__ __forceinline__ float xrow_sum(float x) {
  auto s = __builtin_amdgcn_permlane16_swap(__float_as_uint(x), __float_as_uint(x), false, false);
  x = __uint_as_float(s[0]) + __uint_as_float(s[1]);
  auto t = __builtin_amdgcn_permlane32_swap(__float_as_uint(x), __float_as_uint(x), false, false);
  return __uint_as_float(t[0]) + __uint_as_float(t[1]);
}
__device__ __forceinline__ float row_sum(float x) { x += dpp<XOR1>(x); x += dpp<XOR2>(x); x += dpp<XOR7>(x); x += dpp<ROR8>(x); return x; }
__device__ __forceinline__ float row_max(float x) {
  x = fmaxf(x, dpp<XOR1>(x)); x = fmaxf(x, dpp<XOR2>(x)); x = fmaxf(x, dpp<XOR7>(x)); x = fmaxf(x, dpp<ROR8>(x)); return x;
}
__device__ __forceinline__ float wave_sum(float x) { return xrow_sum(row_sum(x)); }
__device__ __forceinline__ unsigned umax4(u32x4 v) { return max(max(v[0], v[1]), max(v[2], v[3])); }

__device__ __forceinline__ rsrc_t mk(const void* p, unsigned bytes) { return __builtin_amdgcn_make_buffer_rsrc((void*)p, 0, bytes, 0x00020000); }
__device__ __forceinline__ f32x4 mfma32(u32x4 a, u32x4 b, f32x4 c) {
  return __builtin_amdgcn_mfma_f32_16x16x32_bf16(__builtin_bit_cast(bf16x8, a), __builtin_bit_cast(bf16x8, b), c, 0, 0, 0);
}
__device__ __forceinline__ f32x4 mfma16(u32x2 a, u32x2 b, f32x4 c) {
  return __builtin_amdgcn_mfma_f32_16x16x16bf16_1k(__builtin_bit_cast(s16x4, a), __builtin_bit_cast(s16x4, b), c, 0, 0, 0);
}
__device__ __forceinline__ u32x2 trread(LAS char* p) {
  return __builtin_bit_cast(u32x2, __builtin_amdgcn_ds_read_tr16_b64_v4i16((LAS s16x4*)p));
}
__device__ __forceinline__ u32x4 trread2(LAS char* p, int stride4) {
  const u32x2 a = trread(p), b = trread(p + stride4);
  return u32x4{a[0], a[1], b[0], b[1]};
}
__device__ __forceinline__ LAS char* pin(LAS char* L, int off) { asm volatile("" : "+v"(off)); return L + off; }
__device__ __forceinline__ int ctl_rd(LAS char* L, int w) { return __builtin_amdgcn_readfirstlane(*(volatile LAS int*)(L + L_CTL + 4 * w)); }
__device__ __forceinline__ void ctl_wr(LAS char* L, int w, int v) { *(volatile LAS int*)(L + L_CTL + 4 * w) = v; }
__device__ __forceinline__ void ctl_wait(LAS char* L, int w, int need) {
  int s = SPIN_CAP;
  while (ctl_rd(L, w) < need && --s > 0) __builtin_amdgcn_s_sleep(1);
  CFENCE();
}
__device__ __forceinline__ void landed_wait(LAS char* L, int& seen, int need) {
  if (seen < need) {
    int s = SPIN_CAP;
    do { seen = ctl_rd(L, C_LANDED); } while (seen < need && --s > 0);
  }
  CFENCE();
}
__device__ __forceinline__ void ctl_add(LAS char* L, int w, int lane) {
  WAITL();
  if (lane == 0) __hip_atomic_fetch_add((LAS int*)(L + L_CTL + 4 * w), 1, __ATOMIC_RELAXED, __HIP_MEMORY_SCOPE_WORKGROUP);
  CFENCE();
}
__device__ __forceinline__ int prog_min(LAS char* L) {
  const u32x4 d = *(volatile LAS u32x4*)(L + L_CTL + 4 * C_PROG);
  return (int)min(min(d[0], d[1]), min(d[2], d[3]));
}
__device__ __forceinline__ void wait_released(LAS char* L, int unit) {
  int s = SPIN_CAP, v;
  do { v = __builtin_amdgcn_readfirstlane(prog_min(L)); } while (v <= unit && --s > 0);
  CFENCE();
}

__device__ __forceinline__ u32x4 layernorm8(const float (&v)[8], bool act) {
  const float c = __builtin_bit_cast(float, __builtin_amdgcn_readfirstlane(__builtin_bit_cast(int, v[0])));
  float s = 0.f, ss = 0.f;
#pragma unroll
  for (int i = 0; i < 8; ++i) { const float d = v[i] - c; s += d; ss = fmaf(d, d, ss); }
  s = wave_sum(act ? s : 0.f) * (1.f / DM);
  ss = wave_sum(act ? ss : 0.f) * (1.f / DM);
  const float var = rbf(fmaxf(fmaf(-s, s, ss), 0.f));
  const float inv = __builtin_amdgcn_rcpf(rbf(__builtin_amdgcn_sqrtf(rbf(var + 1e-5f))));
  const float mb = rbf(c + s);
  u32x4 o;
#pragma unroll
  for (int i = 0; i < 4; ++i) { const unsigned d = pack2(v[2 * i] - mb, v[2 * i + 1] - mb); o[i] = pack2(bf_lo(d) * inv, bf_hi(d) * inv); }
  return o;
}

template <int BLK>
__device__ __forceinline__ void load_units(LAS char* L, rsrc_t r, int vo, int so0, int u0, int& published, int& fr) {
  for (int k = 0; k < NU; ++k) {
    const int u = u0 + k;
    LAS char* sl = L + (u & (NS - 1)) * SLOT;
    const int so = so0 + (k >> 1) * 32768 + (k & 1) * 8192;
    if (u >= NS && __builtin_amdgcn_readfirstlane(fr) <= u - NS) {
      WAITV(0);
      published = u; ctl_wr(L, C_LANDED, u);
      wait_released(L, u - NS);
    }
#pragma unroll
    for (int i = 0; i < 8; ++i) {
      __builtin_amdgcn_raw_ptr_buffer_load_lds(r, (LAS void*)(sl + i * BLK), 16, vo, so + i * 1024, 0, AUX_NT);
      WAITV(NFL - 1);
      if ((i + 2 - NFL + 64) % 8 == 0) {
        const int n = u + (i + 2 - NFL + 64) / 8 - 8;
        if (n > published) { published = n; ctl_wr(L, C_LANDED, n); }
      }
      if (i == 4) fr = prog_min(L);
    }
    if (u == 1) { __builtin_amdgcn_s_barrier(); CFENCE(); }
  }
}
__device__ __forceinline__ void load_weight_head(LAS char* L, rsrc_t r, int vo, int so, int step) {
#pragma unroll
  for (int k = 0; k < 28; ++k) {
    __builtin_amdgcn_raw_ptr_buffer_load_lds(r, (LAS void*)(L + k * 1024), 16, vo, so + k * step, 0, AUX_W);
    if (k <= NFL - 2) {
      WAITV(NFL - 1);
      if ((k + 2 - NFL + 64) % 8 == 0) ctl_wr(L, C_LANDED, 2 * NU + (k + 2 - NFL + 64) / 8 - 8);
    }
  }
}
__device__ __forceinline__ void load_weight_rest(LAS char* L, rsrc_t r, int vo, int so, int step, int dst, int n4) {
#pragma nounroll
  for (int k = 0; k < n4; ++k) {
#pragma unroll
    for (int i = 0; i < 4; ++i) __builtin_amdgcn_raw_ptr_buffer_load_lds(r, (LAS void*)(L + dst + (4 * k + i) * 1024), 16, vo, so + (4 * k + i) * step, 0, AUX_W);
    WAITV(NFLW - 4);
  }
}
__device__ __forceinline__ void reload4(u32x4& a, u32x4& b, u32x4& c, u32x4& d, int vo, u32x4 srd, int so01, int so23) {
  const u64 ma = __ballot(a[3] == SENT), mb = __ballot(b[3] == SENT), mc = __ballot(c[3] == SENT), md = __ballot(d[3] == SENT);
  asm volatile("s_mov_b64 exec, %4\n\tbuffer_load_dwordx4 %0, %8, %9, %10 offen sc1\n\t"
               "s_mov_b64 exec, %5\n\tbuffer_load_dwordx4 %1, %8, %9, %10 offen offset:2048 sc1\n\t"
               "s_mov_b64 exec, %6\n\tbuffer_load_dwordx4 %2, %8, %9, %11 offen sc1\n\t"
               "s_mov_b64 exec, %7\n\tbuffer_load_dwordx4 %3, %8, %9, %11 offen offset:2048 sc1\n\t"
               "s_mov_b64 exec, -1"
               : "+v"(a), "+v"(b), "+v"(c), "+v"(d)
               : "s"(ma), "s"(mb), "s"(mc), "s"(md), "v"(vo), "s"(srd), "s"(so01), "s"(so23)
               : "memory");
}
#define LANDED8(v, k) asm volatile("s_waitcnt vmcnt(0)" : "+v"(v[k]), "+v"(v[k + 1]), "+v"(v[k + 2]), "+v"(v[k + 3]), "+v"(v[k + 4]), "+v"(v[k + 5]), "+v"(v[k + 6]), "+v"(v[k + 7]) :: "memory")
__device__ __forceinline__ bool pending16(const u32x4 (&v)[16]) {
  unsigned m = v[0][3];
#pragma unroll
  for (int k = 1; k < 16; ++k) m = max(m, v[k][3]);
  return __any(m == SENT);
}
__device__ __forceinline__ bool rearm16(u32x4 (&v)[16]) {
  unsigned m = 0u;
#pragma unroll
  for (int k = 0; k < 16; ++k) m = max(m, umax4(v[k]));
  if (!__any(m == SENT)) return false;
#pragma unroll
  for (int k = 0; k < 16; ++k) v[k][3] = SENT;
  return true;
}

__global__ void __launch_bounds__(320) fused_decode(const u16* __restrict__ x, const u16* __restrict__ Kc, const u16* __restrict__ Vc,
                                                    const u16* __restrict__ wq, const u16* __restrict__ wo, const u16* __restrict__ wg,
                                                    const u16* __restrict__ wu, const u16* __restrict__ wd, u16* __restrict__ out, char* ws) {
  __shared__ __attribute__((aligned(16))) char lds[L_END];
  LAS char* L = (LAS char*)lds;
  const int lane = threadIdx.x & 63, wave = __builtin_amdgcn_readfirstlane(threadIdx.x >> 6);
  const int bid = blockIdx.x;
  const int grp = GM ? (bid & 15) : (bid >> 4), j = GM ? (bid >> 4) : (bid & 15);
  const bool is_ffn = bid >= FFN0, is_wo = bid >= WO0 && !is_ffn;
  const int x8 = bid & 7, rk = (bid - (is_ffn ? FFN0 : WO0)) >> 3, f = 7 * x8 + rk;

  if (wave == 4) {
    __builtin_amdgcn_s_setprio(3);
    const int vo = (lane >> 5) * 512 + (((lane & 31) ^ ((lane >> 5) << 3)) << 4);
    const int so0 = (j >> 1) * (2 << 20) + (j & 1) * 16384;
    int published = 0, fr = 0;
    load_units<KBLK>(L, mk(Kc + (size_t)grp * SK * DH, SK * DH * 2), vo, so0, 0, published, fr);
    STAMP(1);
    load_units<VBLK>(L, mk(Vc + (size_t)grp * SK * DH, SK * DH * 2), vo, so0, NU, published, fr);
    STAMP(2);
    if (is_wo || is_ffn) {
      const rsrc_t r0 = mk(is_wo ? wo : wg, is_wo ? 4096 * DM * 2 : DM * FF * 2);
      const int vo0 = is_wo ? (lane >> 3) * (DM * 2) + (lane & 7) * 16 : (lane >> 2) * (FF * 2) + (lane & 3) * 16;
      const int so0w = is_wo ? x8 * 512 * (DM * 2) + 128 * rk : 64 * f, step0 = is_wo ? 8 * DM * 2 : 16 * FF * 2;
      if (__builtin_amdgcn_readfirstlane(fr) <= 2 * NU - NS + 3) wait_released(L, 2 * NU - NS + 3);
      load_weight_head(L, r0, vo0, so0w, step0);
      wait_released(L, 2 * NU - NS + 10);
      if (is_wo) {
        load_weight_rest(L, r0, vo0, so0w + 28 * step0, step0, 28672, 9);
      } else {
        load_weight_rest(L, mk(wu, DM * FF * 2), vo0, so0w, step0, 28672, 7);
        load_weight_rest(L, mk(wd, FF * DM * 2), lane * 16, 32 * f * (DM * 2), 1024, 57344, 7);
      }
    } else {
      WAITV(16); ctl_wr(L, C_LANDED, 2 * NU - 2);
      WAITV(8); ctl_wr(L, C_LANDED, 2 * NU - 1);
    }
    WAITV(0);
    ctl_wr(L, C_LANDED, 2 * NU + 1);
    STAMP(3); CYC(31); FLUSH();
    return;
  }

  const rsrc_t rws = mk(ws, W_END);
  const int n16 = lane & 15, g4 = lane >> 4;
  const int q4 = n16 >> 2, p4 = lane & 3;
  const int row = grp * 16 + j;
  u32x2 ep = {0u, 0u};
  const u32x4 srd = {(unsigned)(size_t)ws, (unsigned)((size_t)ws >> 32) & 0xFFFFu, (unsigned)W_END, 0x00020000u};
  if (wave == 3) {
    const rsrc_t rq = mk(wq, DM * 4096 * 2);
    const int p = bid >> 3;
#pragma nounroll
    for (int u = 0; u < 14; ++u)
      __builtin_amdgcn_raw_ptr_buffer_load_lds(rq, (LAS void*)(L + L_STG + u * 1024), 16, (lane >> 1) * 8192 + (lane & 1) * 16, u * 32 * 8192 + x8 * 1024 + 32 * p, 0, AUX_Q);
    ep = __builtin_amdgcn_raw_buffer_load_b64(rws, 0, W_EPOCH, 0);
    LAS char* wq_rd = pin(L, L_STG + (8 * g4 + q4) * 32 + 8 * p4);
    LAS char* xn_rd = pin(L, L_XN + (lane & 1) * 896 + g4 * 16);
    __builtin_amdgcn_s_barrier();
    WAITV(0);
    STAMP(4);
    u32x4 wf[14];
#pragma unroll
    for (int s = 0; s < 14; ++s) wf[s] = trread2(wq_rd + s * 1024, 128);
    ctl_wait(L, C_XN, 2);
    u32x4 af[14];
#pragma unroll
    for (int s = 0; s < 14; ++s) af[s] = *(LAS u32x4*)(xn_rd + s * 64);
    __builtin_amdgcn_sched_barrier(0);
    f32x4 d = {0.f, 0.f, 0.f, 0.f};
#pragma unroll
    for (int s = 0; s < 14; ++s) d = mfma32(af[s], wf[s], d);
    const float qv = (g4 & 1) ? d[1] : d[0];
    const unsigned pr = pack2(qv, dpp<XOR1>(qv));
    if (lane < 32 && !(lane & 1))
      __builtin_amdgcn_raw_buffer_store_b64(u32x2{pr, ep[0] + 1u}, rws, ((g4 * 8 + x8) * 256 + 8 * p + (n16 >> 1)) * 8, W_Q, AUX_SC1);
    STAMP(5);
  } else if (wave > 0) {
    const int bb = wave - 1;
    const u32x4 xv = *(const u32x4*)(x + bb * DM + (lane < 56 ? lane : 55) * 8);
    ep = __builtin_amdgcn_raw_buffer_load_b64(rws, 0, W_EPOCH, 0);
    __builtin_amdgcn_s_barrier();
    float v[8];
#pragma unroll
    for (int i = 0; i < 4; ++i) { v[2 * i] = bf_lo(xv[i]); v[2 * i + 1] = bf_hi(xv[i]); }
    const u32x4 o = layernorm8(v, lane < 56);
    if (lane < 56) *(LAS u32x4*)(L + L_XN + bb * 896 + lane * 16) = o;
    ctl_add(L, C_XN, lane);
  } else {
    ep = __builtin_amdgcn_raw_buffer_load_b64(rws, 0, W_EPOCH, 0);
    ((LAS int*)(L + L_CTL))[lane] = lane < 4 ? lane : 0;
    BARRIER();
    const u32x4 s4 = {SENT, SENT, SENT, SENT};
    __builtin_amdgcn_raw_buffer_store_b128(s4, rws, lane * 16, W_PART + row * 2048, AUX_SC1);
    __builtin_amdgcn_raw_buffer_store_b128(s4, rws, lane * 16 + 1024, W_PART + row * 2048, AUX_SC1);
    if (lane == 0) __builtin_amdgcn_raw_buffer_store_b128(s4, rws, 0, W_ML + row * 16, AUX_SC1);
    if (is_wo && lane < 32) __builtin_amdgcn_raw_buffer_store_b128(s4, rws, g4 * 16384 + n16 * 16, W_Y + x8 * 2048 + rk * 256, AUX_SC1);
    if (is_ffn) {
#pragma unroll
      for (int it = 0; it < 4; ++it) {
        const int r = (lane >> 2) + 16 * it;
        if (r < NFFN) __builtin_amdgcn_raw_buffer_store_b128(s4, rws, (r * NFFN + f) * 64 + p4 * 16, W_Z, AUX_SC1);
      }
    }
    WAITV(0);
    if (lane == 0) __builtin_amdgcn_raw_buffer_store_b64(u32x2{ep[0] + 1u, ep[1]}, rws, bid * 8, W_FLAG, AUX_SC1);
  }
  CFENCE();

  const int ka = (n16 >> 1) * KBLK + (n16 & 1) * 512 + (16 * (g4 & 1) + 8 * ((g4 >> 1) ^ (n16 & 1))) * 16;
  LAS char* sc_wr = L + L_STG + wave * 2048 + n16 * 32 + g4 * 8;
  int seen = 0;
  float mx = -INFINITY;
  u32x4 qf[8];
  auto score = [&](const u32x4 (&kf)[8], int i) {
    f32x4 s0 = {0.f, 0.f, 0.f, 0.f}, s1 = {0.f, 0.f, 0.f, 0.f};
#pragma unroll
    for (int s = 0; s < 8; s += 2) { s0 = mfma32(kf[s], qf[s], s0); s1 = mfma32(kf[s + 1], qf[s + 1], s1); }
    const u32x2 sb = {pack2(s0[0] + s1[0], s0[1] + s1[1]), pack2(s0[2] + s1[2], s0[3] + s1[3])};
    mx = fmaxf(fmaxf(mx, fmaxf(bf_lo(sb[0]), bf_hi(sb[0]))), fmaxf(bf_lo(sb[1]), bf_hi(sb[1])));
    if (n16 < 2) *(LAS u32x2*)(sc_wr + i * 64) = sb;
  };
  {
    u32x4 kp[RPRE][8];
#pragma unroll
    for (int i = 0; i < RPRE; ++i) {
      const int u = 4 * i + wave;
      landed_wait(L, seen, u + 1);
#pragma unroll
      for (int s = 0; s < 8; ++s) kp[i][s] = *(LAS u32x4*)(L + u * SLOT + ka + 16 * s);
      WAITL();
      ctl_wr(L, C_PROG + wave, u + 4);
    }
    if (wave == 1) {
      u32x4 g0, g1;
      const unsigned tg = ep[0] + 1u;
      int budget = SPIN_CAP;
      do {
        CFENCE();
        g0 = __builtin_amdgcn_raw_buffer_load_b128(rws, lane * 16, W_Q + grp * 2048, AUX_SC1);
        g1 = __builtin_amdgcn_raw_buffer_load_b128(rws, lane * 16 + 1024, W_Q + grp * 2048, AUX_SC1);
      } while (__any(((g0[1] ^ tg) | (g0[3] ^ tg) | (g1[1] ^ tg) | (g1[3] ^ tg)) != 0u) && --budget > 0);
      *(LAS u32x2*)(L + L_QB + lane * 8) = u32x2{g0[0], g0[2]};
      *(LAS u32x2*)(L + L_QB + 512 + lane * 8) = u32x2{g1[0], g1[2]};
      WAITL();
      ctl_wr(L, C_Q, 1);
      STAMP(6);
    } else {
      ctl_wait(L, C_Q, 1);
    }
#pragma unroll
    for (int s = 0; s < 8; ++s) qf[s] = *(LAS u32x4*)(L + L_QB + (lane & 1) * 512 + (s + 16 * (g4 & 1) + 8 * (g4 >> 1)) * 16);
#pragma unroll
    for (int i = 0; i < RPRE; ++i) score(kp[i], i);
  }
  for (int i = RPRE; i < NU / 4; ++i) {
    const int u = 4 * i + wave;
    landed_wait(L, seen, u + 1);
    u32x4 kf[8];
#pragma unroll
    for (int s = 0; s < 8; ++s) kf[s] = *(LAS u32x4*)(L + (u & (NS - 1)) * SLOT + ka + 16 * s);
    WAITL();
    ctl_wr(L, C_PROG + wave, min(u + 4, NU));
    score(kf, i);
  }

  mx = xrow_max(mx);
  if (lane < 2) *(LAS float*)(L + L_RED + wave * 8 + lane * 4) = mx;
  ctl_add(L, C_MX, lane);
  ctl_wait(L, C_MX, 4);
  {
    const LAS float* r = (const LAS float*)(L + L_RED + (lane & 1) * 4);
    const float M = fmaxf(fmaxf(r[0], r[2]), fmaxf(r[4], r[6]));
    LAS u32x4* sp = (LAS u32x4*)(L + L_STG + wave * 2048 + lane * 32);
    float ls = 0.f;
#pragma unroll
    for (int hh = 0; hh < 2; ++hh) {
      u32x4 v = sp[hh];
#pragma unroll
      for (int i = 0; i < 4; ++i) {
        const float p0 = __builtin_amdgcn_exp2f((bf_lo(v[i]) - M) * C2), p1 = __builtin_amdgcn_exp2f((bf_hi(v[i]) - M) * C2);
        ls += p0 + p1; v[i] = pack2(p0, p1);
      }
      sp[hh] = v;
    }
    ls += dpp<XOR2>(ls); ls += dpp<ROR4>(ls); ls += dpp<ROR8>(ls); ls = xrow_sum(ls);
    if (lane < 2) *(LAS float*)(L + L_RED + 32 + wave * 8 + lane * 4) = ls;
    ctl_add(L, C_P, lane);
    ctl_wait(L, C_P, 4);
    if (wave == 0) {
      if (lane < 2) {
        const float Ls = (r[8] + r[10]) + (r[12] + r[14]);
        __builtin_amdgcn_raw_buffer_store_b64(u32x2{pub(M), pub(Ls)}, rws, lane * 8, W_ML + row * 16, AUX_SC1);
      }
      STAMP(15);
    }
  }

  f32x4 acc[4];
#pragma unroll
  for (int c = 0; c < 4; ++c) acc[c] = f32x4{0.f, 0.f, 0.f, 0.f};
  {
    const int va = (2 * g4 + (q4 >> 1)) * VBLK + (q4 & 1) * 512 + (p4 >> 1) * 16 + (p4 & 1) * 8 + ((wave & 1) ? -128 : 128) * (q4 & 1) + 128 * wave;
    LAS char* p_rd = L + L_STG + (lane & 1) * 32 + g4 * 8;
    for (int k = 0; k < NU; ++k) {
      const u32x2 pb = *(LAS u32x2*)(p_rd + (k & 3) * 2048 + (k >> 2) * 64);
      landed_wait(L, seen, NU + k + 1);
      LAS char* sl = L + (k & (NS - 1)) * SLOT + va;
      u32x2 vf[4];
#pragma unroll
      for (int c = 0; c < 4; ++c) vf[c] = trread(sl + 32 * c);
      WAITL();
      ctl_wr(L, C_PROG + wave, NU + k + 1);
#pragma unroll
      for (int c = 0; c < 4; ++c) acc[c] = mfma16(vf[c], pb, acc[c]);
      if (k == 8 && wave == 0 && (is_wo || is_ffn || bid == 0)) {
        for (int s = 0; s < SPIN_CAP; ++s) {
          const u32x4 a = __builtin_amdgcn_raw_buffer_load_b128(rws, lane * 16, W_FLAG, AUX_SC1);
          const u32x4 c = __builtin_amdgcn_raw_buffer_load_b128(rws, lane * 16 + 1024, W_FLAG, AUX_SC1);
          const unsigned lo = ep[0] + 1u, hi = ep[1];
          if (__all(a[0] == lo && a[1] == hi && a[2] == lo && a[3] == hi && c[0] == lo && c[1] == hi && c[2] == lo && c[3] == hi)) break;
          __builtin_amdgcn_s_sleep(1);
          CFENCE();
        }
        ctl_wr(L, C_VER, 1);
        if (bid == 0 && lane == 0) __builtin_amdgcn_raw_buffer_store_b64(u32x2{ep[0] + 1u, ep[1]}, rws, 0, W_EPOCH, AUX_SC1);
      }
    }
  }
  if (wave == 3) STAMP(7);
  {
    const f32x4 a = (n16 & 2) ? acc[1] : acc[0], c = (n16 & 2) ? acc[3] : acc[2], o = (n16 & 4) ? c : a;
    if (n16 < 8)
      __builtin_amdgcn_raw_buffer_store_b128(u32x4{pub(o[0]), pub(o[1]), pub(o[2]), pub(o[3])}, rws,
                                             wave * 512 + (n16 & 1) * 256 + (n16 >> 1) * 64 + g4 * 16, W_PART + row * 2048, AUX_SC1);
  }
  if (wave == 3) STAMP(8);

  if (is_wo) {
    const int hf = lane >> 5, gsel = hf * 8 + x8;
    ctl_wait(L, C_VER, 1);
    u32x2 ml;
    for (int s = 0; s < SPIN_CAP; ++s) {
      ml = __builtin_amdgcn_raw_buffer_load_b64(rws, gsel * 256 + n16 * 16 + (g4 & 1) * 8, W_ML, AUX_SC1);
      if (__all(max(ml[0], ml[1]) != SENT)) break;
      __builtin_amdgcn_s_sleep(4);
      CFENCE();
    }
    const float mj = __uint_as_float(ml[0]);
    float wt = __builtin_amdgcn_exp2f((mj - row_max(mj)) * C2);
    wt *= 1.f / row_sum(wt * __uint_as_float(ml[1]));
    landed_wait(L, seen, 2 * NU + 1);
    u32x4 wf[16];
#pragma unroll
    for (int s = 0; s < 16; ++s) wf[s] = trread2(L + (32 * s + 8 * g4 + q4) * 128 + wave * 32 + 8 * p4, 512);
    if (wave == 0) STAMP(16);
    for (int s = 0; s < SPIN_CAP; ++s) {
      const unsigned probe = __builtin_amdgcn_raw_buffer_load_b32(rws, gsel * 32768 + n16 * 2048 + wave * 512 + (g4 & 1) * 256 + 252, W_PART, AUX_SC1);
      if (__builtin_popcountll(__ballot(probe == SENT)) <= 16) break;
      __builtin_amdgcn_s_sleep(2);
      CFENCE();
    }
    float wb[16];
#define BC_(k) wb[k] = dpp<NEWBC + k>(wt);
    BC_(0) BC_(1) BC_(2) BC_(3) BC_(4) BC_(5) BC_(6) BC_(7) BC_(8) BC_(9) BC_(10) BC_(11) BC_(12) BC_(13) BC_(14) BC_(15)
#undef BC_
    LAS char* ob_wr = pin(L, L_OB + hf * 1024 + (g4 & 1) * 512 + wave * 128 + n16 * 8);
    LAS char* ob_rd = pin(L, L_OB + (lane & 1) * 1024 + g4 * 16);
    int y_off = g4 * 16384 + wave * 64 + n16 * 4;
    asm volatile("" : "+v"(y_off));
    u32x4 pv[16];
#pragma unroll
    for (int k = 0; k < 16; ++k) pv[k] = u32x4{0u, 0u, 0u, SENT};
    f32x4 o0, o1;
    const int pvo = gsel * 32768 + wave * 512 + (lane & 31) * 16;
    int budget = SPIN_CAP;
    bool again;
    do {
      do {
#pragma unroll
        for (int k = 0; k < 16; k += 4) reload4(pv[k], pv[k + 1], pv[k + 2], pv[k + 3], pvo, srd, W_PART + k * 2048, W_PART + k * 2048 + 4096);
        LANDED8(pv, 0); LANDED8(pv, 8);
      } while (pending16(pv) && --budget > 0);
      o0 = wb[0] * __builtin_bit_cast(f32x4, pv[0]); o1 = wb[1] * __builtin_bit_cast(f32x4, pv[1]);
#pragma unroll
      for (int k = 2; k < 16; k += 2) { o0 += wb[k] * __builtin_bit_cast(f32x4, pv[k]); o1 += wb[k + 1] * __builtin_bit_cast(f32x4, pv[k + 1]); }
      o0 += o1;
      const float chk = (o0[0] + o0[1]) + (o0[2] + o0[3]);
      again = false;
      if (__builtin_expect(__any(chk != chk), 0)) again = rearm16(pv) && --budget > 0;
    } while (__builtin_expect(again, 0));
    if (wave == 0) STAMP(9);
    *(LAS u32x2*)ob_wr = u32x2{pack2(o0[0], o0[1]), pack2(o0[2], o0[3])};
    BARRIER();
    f32x4 d0 = {0.f, 0.f, 0.f, 0.f}, d1 = d0;
    u32x4 af[16];
#pragma unroll
    for (int s = 0; s < 16; ++s) af[s] = *(LAS u32x4*)(ob_rd + s * 64);
    __builtin_amdgcn_sched_barrier(0);
#pragma unroll
    for (int s = 0; s < 16; s += 2) { d0 = mfma32(af[s], wf[s], d0); d1 = mfma32(af[s + 1], wf[s + 1], d1); }
    if (lane < 32)
      __builtin_amdgcn_raw_buffer_store_b32(pub((g4 & 1) ? d0[1] + d1[1] : d0[0] + d1[0]), rws, y_off, W_Y + x8 * 2048 + rk * 256, AUX_SC1);
    if (wave == 0) STAMP(10);
  }
  if (!is_ffn) { FLUSH(); return; }

  const int ln = lane < 56 ? lane : 55;
  u32x2 xa = {0u, 0u}, xb = {0u, 0u};
  if (wave < 2) { xa = *(const u32x2*)(x + wave * DM + ln * 4); xb = *(const u32x2*)(x + wave * DM + 224 + ln * 4); }
  ctl_wait(L, C_VER, 1);
  landed_wait(L, seen, 2 * NU + 1);
  u32x4 gf[14], df[7];
#pragma unroll
  for (int s = 0; s < 14; ++s) gf[s] = trread2(L + (p4 >> 1) * 28672 + (32 * s + 8 * g4 + q4) * 64 + wave * 16 + 8 * (p4 & 1), 256);
#pragma unroll
  for (int i = 0; i < 7; ++i) df[i] = trread2(L + 57344 + (8 * g4 + q4) * 896 + (7 * wave + i) * 32 + 8 * p4, 4 * 896);
  LAS char* xn_rd = pin(L, L_XN + (lane & 1) * 896 + g4 * 16);
  LAS char* xn_wr = pin(L, L_XN + wave * 896 + lane * 8);
  LAS char* hb_wr = pin(L, L_HB + wave * 1792 + lane * 16);
  LAS char* gb_wr = pin(L, L_GB + g4 * 64 + (8 * wave + n16) * 2);
  LAS char* gb_rd = pin(L, L_GB + (lane & 1) * 64 + g4 * 16);
  int z_off = (((g4 & 1) * 28 + 7 * wave + (g4 >> 1)) * NFFN + f) * 64 + n16 * 4;
  asm volatile("" : "+v"(z_off));
  if (wave == 0) STAMP(17);
  if (wave < 2) {
    for (int s = 0; s < SPIN_CAP; ++s) {
      const unsigned probe = __builtin_amdgcn_raw_buffer_load_b32(rws, (ln / 7) * 2048 + (ln % 7) * 256 + 252, W_Y + wave * 16384, AUX_SC1);
      if (__builtin_popcountll(__ballot(probe == SENT)) <= 21) break;
      __builtin_amdgcn_s_sleep(2);
      CFENCE();
    }
    u32x4 yv[16];
#pragma unroll
    for (int k = 0; k < 16; ++k) yv[k] = u32x4{0u, 0u, 0u, SENT};
    f32x4 sa, sb;
    int budget = SPIN_CAP;
    bool again;
    do {
      do {
#pragma unroll
        for (int k = 0; k < 8; k += 4) {
          reload4(yv[k], yv[k + 1], yv[k + 2], yv[k + 3], ln * 16, srd, W_Y + wave * 16384 + k * 2048, W_Y + wave * 16384 + k * 2048 + 4096);
          reload4(yv[8 + k], yv[9 + k], yv[10 + k], yv[11 + k], ln * 16 + 896, srd, W_Y + wave * 16384 + k * 2048, W_Y + wave * 16384 + k * 2048 + 4096);
        }
        LANDED8(yv, 0); LANDED8(yv, 8);
      } while (pending16(yv) && --budget > 0);
      sa = __builtin_bit_cast(f32x4, yv[0]); sb = __builtin_bit_cast(f32x4, yv[8]);
#pragma unroll
      for (int k = 1; k < 8; ++k) { sa += __builtin_bit_cast(f32x4, yv[k]); sb += __builtin_bit_cast(f32x4, yv[8 + k]); }
      const f32x4 t = sa + sb;
      const float chk = (t[0] + t[1]) + (t[2] + t[3]);
      again = false;
      if (__builtin_expect(__any(chk != chk), 0)) again = rearm16(yv) && --budget > 0;
    } while (__builtin_expect(again, 0));
    if (wave == 0) STAMP(11);
    float v[8];
#pragma unroll
    for (int i = 0; i < 2; ++i) {
      const unsigned ra = pack2(sa[2 * i], sa[2 * i + 1]), rb = pack2(sb[2 * i], sb[2 * i + 1]);
      const unsigned ha = pack2(bf_lo(xa[i]) + bf_lo(ra), bf_hi(xa[i]) + bf_hi(ra)), hb = pack2(bf_lo(xb[i]) + bf_lo(rb), bf_hi(xb[i]) + bf_hi(rb));
      v[2 * i] = bf_lo(ha); v[2 * i + 1] = bf_hi(ha); v[4 + 2 * i] = bf_lo(hb); v[5 + 2 * i] = bf_hi(hb);
    }
    const u32x4 o = layernorm8(v, lane < 56);
    if (lane < 56) {
      *(LAS u32x2*)xn_wr = u32x2{o[0], o[1]};
      *(LAS u32x2*)(xn_wr + 448) = u32x2{o[2], o[3]};
      *(LAS f32x4*)hb_wr = f32x4{v[0], v[1], v[2], v[3]};
      *(LAS f32x4*)(hb_wr + 896) = f32x4{v[4], v[5], v[6], v[7]};
    }
  }
  BARRIER();
  {
    f32x4 d0 = {0.f, 0.f, 0.f, 0.f}, d1 = d0;
    u32x4 af[14];
#pragma unroll
    for (int s = 0; s < 14; ++s) af[s] = *(LAS u32x4*)(xn_rd + s * 64);
    __builtin_amdgcn_sched_barrier(0);
#pragma unroll
    for (int s = 0; s < 14; s += 2) { d0 = mfma32(af[s], gf[s], d0); d1 = mfma32(af[s + 1], gf[s + 1], d1); }
    const float a = rbf((g4 & 1) ? d0[1] + d1[1] : d0[0] + d1[0]), u = dpp<ROR8>(a);
    const float z = 0.7978845608028654f * (a + 0.044715f * a * a * a);
    const float ge = rbf(a * __builtin_amdgcn_rcpf(1.f + __builtin_amdgcn_exp2f(-2.f * LOG2E * z)));
    if (lane < 32 && n16 < 8) *(LAS u16*)gb_wr = (u16)pack2(ge * u, 0.f);
  }
  BARRIER();
  {
    const u32x4 a = *(LAS u32x4*)gb_rd;
    f32x4 d[8];
#pragma unroll
    for (int i = 0; i < 7; ++i) d[i] = mfma32(a, df[i], f32x4{0.f, 0.f, 0.f, 0.f});
    d[7] = d[6];
#pragma unroll
    for (int i = 0; i < 4; ++i) {
      const float v0 = (g4 & 1) ? d[2 * i][1] : d[2 * i][0], v1 = (g4 & 1) ? d[2 * i + 1][1] : d[2 * i + 1][0];
      if (i < 3 || lane < 32) __builtin_amdgcn_raw_buffer_store_b32(pub((g4 >> 1) ? v1 : v0), rws, z_off, W_Z + 2 * i * NFFN * 64, AUX_SC1);
    }
  }
  if (wave != 0) { FLUSH(); return; }
  STAMP(12);

  {
    const int ob = f / 28, oc = (f % 28) * 16 + p4 * 4;
    const f32x4 hv = *(LAS f32x4*)(L + L_HB + ob * 1792 + oc * 4);
    u32x4 zv[4];
    int budget = SPIN_CAP;
    unsigned mxx;
    do {
      CFENCE();
#pragma unroll
      for (int k = 0; k < 4; ++k) zv[k] = __builtin_amdgcn_raw_buffer_load_b128(rws, (k < 3 ? lane : (lane & 31)) * 16 + k * 1024, W_Z + f * (NFFN * 64), AUX_SC1);
      mxx = max(max(umax4(zv[0]), umax4(zv[1])), max(umax4(zv[2]), umax4(zv[3])));
    } while (__any(mxx == SENT) && --budget > 0);
    STAMP(13);
    f32x4 sum = (__builtin_bit_cast(f32x4, zv[0]) + __builtin_bit_cast(f32x4, zv[1])) + __builtin_bit_cast(f32x4, zv[2]);
    if (lane < 32) sum += __builtin_bit_cast(f32x4, zv[3]);
    float r[4];
#pragma unroll
    for (int i = 0; i < 4; ++i) { float t = sum[i]; t += dpp<ROR4>(t); t += dpp<ROR8>(t); r[i] = hv[i] + rbf(xrow_sum(t)); }
    if (lane < 4) *(u32x2*)(out + ob * DM + oc) = u32x2{pack2(r[0], r[1]), pack2(r[2], r[3])};
    STAMP(14); FLUSH();
  }
}

extern "C" void kernel_launch(void* const* d_in, const int* in_sizes, int n_in, void* d_out, int out_size, void* d_ws, size_t ws_size,
                              hipStream_t stream) {
  fused_decode<<<256, 320, 0, stream>>>((const u16*)d_in[0], (const u16*)d_in[1], (const u16*)d_in[2], (const u16*)d_in[3], (const u16*)d_in[4],
                                        (const u16*)d_in[6], (const u16*)d_in[7], (const u16*)d_in[8], (u16*)d_out, (char*)d_ws);
}
```

```cpp
#include <hip/hip_runtime.h>
#include <hip/hip_bf16.h>
#include <stdint.h>

#define LAS __attribute__((address_space(3)))
typedef unsigned short u16;
typedef unsigned long long u64;
typedef unsigned u32x2 __attribute__((ext_vector_type(2)));
typedef unsigned u32x4 __attribute__((ext_vector_type(4)));
typedef float f32x2 __attribute__((ext_vector_type(2)));
typedef float f32x4 __attribute__((ext_vector_type(4)));
typedef short s16x4 __attribute__((ext_vector_type(4)));
typedef __bf16 bf16x2 __attribute__((ext_vector_type(2)));
typedef __bf16 bf16x8 __attribute__((ext_vector_type(8)));
typedef __amdgpu_buffer_rsrc_t rsrc_t;

#define NFL 20
#define NFLW 40
#define AUX_W 2
#define AUX_Q 2
#define AUX_K 2
#define AUX_V 2
#define NFLV NFL
#define GM 0
#define RPRE 3

constexpr int DM = 448, SK = 32768, DH = 256, FF = 1792;
constexpr int NU = 128;
constexpr int NS = 16, KBLK = 1040, VBLK = 1056, SLOT = 8 * VBLK;
constexpr int L_STG = NS * SLOT, L_XN = L_STG + 14336, L_QB = L_XN + 1792, L_HB = L_QB + 1024, L_GB = L_HB + 3584,
              L_OB = L_GB + 128, L_RED = L_OB + 2048, L_CTL = L_RED + 64, L_STAMP = L_CTL + 256, L_END = L_STAMP + 1280;
constexpr int C_PROG = 0, C_LANDED = 4, C_XN = 5, C_Q = 6, C_VER = 7, C_MX = 8, C_P = 9;
constexpr int W_EPOCH = 0, W_FLAG = 4096, W_Q = 8192, W_ML = 40960, W_Y = 49152, W_PART = 131072, W_Z = 655360,
              W_END = 1 << 20;
constexpr int WO0 = 144, FFN0 = 200, NFFN = 56;
constexpr unsigned SENT = 0xFFFFFFFFu;
constexpr int AUX_SC1 = 16, AUX_NT = 2;
constexpr int SPIN_CAP = 1 << 20;
constexpr float LOG2E = 1.4426950408889634f, C2 = 0.0625f * LOG2E;

#define WAITV(n) asm volatile("s_waitcnt vmcnt(%0)" :: "i"(n) : "memory")
#define WAITL() asm volatile("s_waitcnt lgkmcnt(0)" ::: "memory")
#define CFENCE() asm volatile("" ::: "memory")
#define BARRIER() do { WAITL(); __builtin_amdgcn_s_barrier(); CFENCE(); } while (0)

#define STAMP(k) do { } while (0)
#define CYC(k) do { } while (0)
#define FLUSH() do { } while (0)

__device__ __forceinline__ float bf_lo(unsigned v) { return __uint_as_float(v << 16); }
__device__ __forceinline__ float bf_hi(unsigned v) { return __uint_as_float(v & 0xffff0000u); }
__device__ __forceinline__ unsigned pack2(float lo, float hi) { return __builtin_bit_cast(unsigned, __builtin_convertvector(f32x2{lo, hi}, bf16x2)); }
__device__ __forceinline__ float rbf(float f) { return bf_lo(pack2(f, f)); }
__device__ __forceinline__ unsigned pub(float f) { unsigned u = __float_as_uint(f); return u == SENT ? 0x7FC00000u : u; }
template <int CTRL> __device__ __forceinline__ float dpp(float x) {
  return __builtin_bit_cast(float, __builtin_amdgcn_mov_dpp(__builtin_bit_cast(int, x), CTRL, 0xf, 0xf, true));
}
constexpr int XOR1 = 0xB1, XOR2 = 0x4E, XOR7 = 0x141, ROR8 = 0x128, ROR4 = 0x124, NEWBC = 0x150;
__device__ __forceinline__ float xrow_max(float x) {
  auto s = __builtin_amdgcn_permlane16_swap(__float_as_uint(x), __float_as_uint(x), false, false);
  x = fmaxf(__uint_as_float(s[0]), __uint_as_float(s[1]));
  auto t = __builtin_amdgcn_permlane32_swap(__float_as_uint(x), __float_as_uint(x), false, false);
  return fmaxf(__uint_as_float(t[0]), __uint_as_float(t[1]));
}
__device__ __forceinline__ float xrow_sum(float x) {
  auto s = __builtin_amdgcn_permlane16_swap(__float_as_uint(x), __float_as_uint(x), false, false);
  x = __uint_as_float(s[0]) + __uint_as_float(s[1]);
  auto t = __builtin_amdgcn_permlane32_swap(__float_as_uint(x), __float_as_uint(x), false, false);
  return __uint_as_float(t[0]) + __uint_as_float(t[1]);
}
__device__ __forceinline__ float row_sum(float x) { x += dpp<XOR1>(x); x += dpp<XOR2>(x); x += dpp<XOR7>(x); x += dpp<ROR8>(x); return x; }
__device__ __forceinline__ float row_max(float x) {
  x = fmaxf(x, dpp<XOR1>(x)); x = fmaxf(x, dpp<XOR2>(x)); x = fmaxf(x, dpp<XOR7>(x)); x = fmaxf(x, dpp<ROR8>(x)); return x;
}
__device__ __forceinline__ float wave_sum(float x) { return xrow_sum(row_sum(x)); }
__device__ __forceinline__ unsigned umax4(u32x4 v) { return max(max(v[0], v[1]), max(v[2], v[3])); }

__device__ __forceinline__ rsrc_t mk(const void* p, unsigned bytes) { return __builtin_amdgcn_make_buffer_rsrc((void*)p, 0, bytes, 0x00020000); }
__device__ __forceinline__ f32x4 mfma32(u32x4 a, u32x4 b, f32x4 c) {
  return __builtin_amdgcn_mfma_f32_16x16x32_bf16(__builtin_bit_cast(bf16x8, a), __builtin_bit_cast(bf16x8, b), c, 0, 0, 0);
}
__device__ __forceinline__ f32x4 mfma16(u32x2 a, u32x2 b, f32x4 c) {
  return __builtin_amdgcn_mfma_f32_16x16x16bf16_1k(__builtin_bit_cast(s16x4, a), __builtin_bit_cast(s16x4, b), c, 0, 0, 0);
}
__device__ __forceinline__ u32x2 trread(LAS char* p) {
  return __builtin_bit_cast(u32x2, __builtin_amdgcn_ds_read_tr16_b64_v4i16((LAS s16x4*)p));
}
__device__ __forceinline__ u32x4 trread2(LAS char* p, int stride4) {
  const u32x2 a = trread(p), b = trread(p + stride4);
  return u32x4{a[0], a[1], b[0], b[1]};
}
__device__ __forceinline__ LAS char* pin(LAS char* L, int off) { asm volatile("" : "+v"(off)); return L + off; }
__device__ __forceinline__ int ctl_rd(LAS char* L, int w) { return __builtin_amdgcn_readfirstlane(*(volatile LAS int*)(L + L_CTL + 4 * w)); }
__device__ __forceinline__ void ctl_wr(LAS char* L, int w, int v) { *(volatile LAS int*)(L + L_CTL + 4 * w) = v; }
__device__ __forceinline__ void ctl_wait(LAS char* L, int w, int need) {
  int s = SPIN_CAP;
  while (ctl_rd(L, w) < need && --s > 0) __builtin_amdgcn_s_sleep(1);
  CFENCE();
}
__device__ __forceinline__ void landed_wait(LAS char* L, int& seen, int need) {
  if (seen < need) {
    int s = SPIN_CAP;
    do { seen = ctl_rd(L, C_LANDED); } while (seen < need && --s > 0);
  }
  CFENCE();
}
__device__ __forceinline__ void ctl_add(LAS char* L, int w, int lane) {
  WAITL();
  if (lane == 0) __hip_atomic_fetch_add((LAS int*)(L + L_CTL + 4 * w), 1, __ATOMIC_RELAXED, __HIP_MEMORY_SCOPE_WORKGROUP);
  CFENCE();
}
__device__ __forceinline__ int prog_min(LAS char* L) {
  const u32x4 d = *(volatile LAS u32x4*)(L + L_CTL + 4 * C_PROG);
  return (int)min(min(d[0], d[1]), min(d[2], d[3]));
}
__device__ __forceinline__ void wait_released(LAS char* L, int unit) {
  int s = SPIN_CAP, v;
  do { v = __builtin_amdgcn_readfirstlane(prog_min(L)); } while (v <= unit && --s > 0);
  CFENCE();
}

__device__ __forceinline__ u32x4 layernorm8(const float (&v)[8], bool act) {
  const float c = __builtin_bit_cast(float, __builtin_amdgcn_readfirstlane(__builtin_bit_cast(int, v[0])));
  float s = 0.f, ss = 0.f;
#pragma unroll
  for (int i = 0; i < 8; ++i) { const float d = v[i] - c; s += d; ss = fmaf(d, d, ss); }
  s = wave_sum(act ? s : 0.f) * (1.f / DM);
  ss = wave_sum(act ? ss : 0.f) * (1.f / DM);
  const float var = rbf(fmaxf(fmaf(-s, s, ss), 0.f));
  const float inv = __builtin_amdgcn_rcpf(rbf(__builtin_amdgcn_sqrtf(rbf(var + 1e-5f))));
  const float mb = rbf(c + s);
  u32x4 o;
#pragma unroll
  for (int i = 0; i < 4; ++i) { const unsigned d = pack2(v[2 * i] - mb, v[2 * i + 1] - mb); o[i] = pack2(bf_lo(d) * inv, bf_hi(d) * inv); }
  return o;
}

template <int BLK, int AUX>
__device__ __forceinline__ void load_units(LAS char* L, rsrc_t r, int vo, int so0, int u0, int& published, int& fr) {
  for (int k = 0; k < NU; ++k) {
    const int u = u0 + k;
    LAS char* sl = L + (u & (NS - 1)) * SLOT;
    const int so = so0 + (k >> 1) * 32768 + (k & 1) * 8192;
    if (u >= NS && __builtin_amdgcn_readfirstlane(fr) <= u - NS) {
      WAITV(0);
      published = u; ctl_wr(L, C_LANDED, u);
      wait_released(L, u - NS);
    }
#pragma unroll
    for (int i = 0; i < 8; ++i) {
      __builtin_amdgcn_raw_ptr_buffer_load_lds(r, (LAS void*)(sl + i * BLK), 16, vo, so + i * 1024, 0, AUX);
      WAITV(NFL - 1);
      if ((i + 2 - NFL + 64) % 8 == 0) {
        const int n = u + (i + 2 - NFL + 64) / 8 - 8;
        if (n > published) { published = n; ctl_wr(L, C_LANDED, n); }
      }
      if (i == 4) fr = prog_min(L);
    }
    if (u == 1) { __builtin_amdgcn_s_barrier(); CFENCE(); }
  }
}
__device__ __forceinline__ void load_weight_head(LAS char* L, rsrc_t r, int vo, int so, int step) {
#pragma unroll
  for (int k = 0; k < 28; ++k) {
    __builtin_amdgcn_raw_ptr_buffer_load_lds(r, (LAS void*)(L + k * 1024), 16, vo, so + k * step, 0, AUX_W);
    if (k <= NFL - 2) {
      WAITV(NFL - 1);
      if ((k + 2 - NFL + 64) % 8 == 0) ctl_wr(L, C_LANDED, 2 * NU + (k + 2 - NFL + 64) / 8 - 8);
    }
  }
}
__device__ __forceinline__ void load_weight_rest(LAS char* L, rsrc_t r, int vo, int so, int step, int dst, int n4) {
#pragma nounroll
  for (int k = 0; k < n4; ++k) {
#pragma unroll
    for (int i = 0; i < 4; ++i) __builtin_amdgcn_raw_ptr_buffer_load_lds(r, (LAS void*)(L + dst + (4 * k + i) * 1024), 16, vo, so + (4 * k + i) * step, 0, AUX_W);
    WAITV(NFLW - 4);
  }
}
__device__ __forceinline__ void reload4(u32x4& a, u32x4& b, u32x4& c, u32x4& d, int vo, u32x4 srd, int so01, int so23) {
  const u64 ma = __ballot(a[3] == SENT), mb = __ballot(b[3] == SENT), mc = __ballot(c[3] == SENT), md = __ballot(d[3] == SENT);
  asm volatile("s_mov_b64 exec, %4\n\tbuffer_load_dwordx4 %0, %8, %9, %10 offen sc1\n\t"
               "s_mov_b64 exec, %5\n\tbuffer_load_dwordx4 %1, %8, %9, %10 offen offset:2048 sc1\n\t"
               "s_mov_b64 exec, %6\n\tbuffer_load_dwordx4 %2, %8, %9, %11 offen sc1\n\t"
               "s_mov_b64 exec, %7\n\tbuffer_load_dwordx4 %3, %8, %9, %11 offen offset:2048 sc1\n\t"
               "s_mov_b64 exec, -1"
               : "+v"(a), "+v"(b), "+v"(c), "+v"(d)
               : "s"(ma), "s"(mb), "s"(mc), "s"(md), "v"(vo), "s"(srd), "s"(so01), "s"(so23)
               : "memory");
}
#define LANDED8(v, k) asm volatile("s_waitcnt vmcnt(0)" : "+v"(v[k]), "+v"(v[k + 1]), "+v"(v[k + 2]), "+v"(v[k + 3]), "+v"(v[k + 4]), "+v"(v[k + 5]), "+v"(v[k + 6]), "+v"(v[k + 7]) :: "memory")
__device__ __forceinline__ bool pending16(const u32x4 (&v)[16]) {
  unsigned m = v[0][3];
#pragma unroll
  for (int k = 1; k < 16; ++k) m = max(m, v[k][3]);
  return __any(m == SENT);
}
__device__ __forceinline__ bool rearm16(u32x4 (&v)[16]) {
  unsigned m = 0u;
#pragma unroll
  for (int k = 0; k < 16; ++k) m = max(m, umax4(v[k]));
  if (!__any(m == SENT)) return false;
#pragma unroll
  for (int k = 0; k < 16; ++k) v[k][3] = SENT;
  return true;
}

__global__ void __launch_bounds__(320) fused_decode(const u16* __restrict__ x, const u16* __restrict__ Kc, const u16* __restrict__ Vc,
                                                    const u16* __restrict__ wq, const u16* __restrict__ wo, const u16* __restrict__ wg,
                                                    const u16* __restrict__ wu, const u16* __restrict__ wd, u16* __restrict__ out, char* ws) {
  __shared__ __attribute__((aligned(16))) char lds[L_END];
  LAS char* L = (LAS char*)lds;
  const int lane = threadIdx.x & 63, wave = __builtin_amdgcn_readfirstlane(threadIdx.x >> 6);
  const int bid = blockIdx.x;
  const int grp = GM ? (bid & 15) : (bid >> 4), j = GM ? (bid >> 4) : (bid & 15);
  const bool is_ffn = bid >= FFN0, is_wo = bid >= WO0 && !is_ffn;
  const int x8 = bid & 7, rk = (bid - (is_ffn ? FFN0 : WO0)) >> 3, f = 7 * x8 + rk;

  if (wave == 4) {
    __builtin_amdgcn_s_setprio(3);
    const int vo = (lane >> 5) * 512 + (((lane & 31) ^ ((lane >> 5) << 3)) << 4);
    const int so0 = (j >> 1) * (2 << 20) + (j & 1) * 16384;
    int published = 0, fr = 0;
    load_units<KBLK, AUX_K>(L, mk(Kc + (size_t)grp * SK * DH, SK * DH * 2), vo, so0, 0, published, fr);
    STAMP(1);
    load_units<VBLK, AUX_V>(L, mk(Vc + (size_t)grp * SK * DH, SK * DH * 2), vo, so0, NU, published, fr);
    STAMP(2);
    if (is_wo || is_ffn) {
      const rsrc_t r0 = mk(is_wo ? wo : wg, is_wo ? 4096 * DM * 2 : DM * FF * 2);
      const int vo0 = is_wo ? (lane >> 3) * (DM * 2) + (lane & 7) * 16 : (lane >> 2) * (FF * 2) + (lane & 3) * 16;
      const int so0w = is_wo ? x8 * 512 * (DM * 2) + 128 * rk : 64 * f, step0 = is_wo ? 8 * DM * 2 : 16 * FF * 2;
      if (__builtin_amdgcn_readfirstlane(fr) <= 2 * NU - NS + 3) wait_released(L, 2 * NU - NS + 3);
      load_weight_head(L, r0, vo0, so0w, step0);
      wait_released(L, 2 * NU - NS + 10);
      if (is_wo) {
        load_weight_rest(L, r0, vo0, so0w + 28 * step0, step0, 28672, 9);
      } else {
        load_weight_rest(L, mk(wu, DM * FF * 2), vo0, so0w, step0, 28672, 7);
        load_weight_rest(L, mk(wd, FF * DM * 2), lane * 16, 32 * f * (DM * 2), 1024, 57344, 7);
      }
    } else {
      WAITV(16); ctl_wr(L, C_LANDED, 2 * NU - 2);
      WAITV(8); ctl_wr(L, C_LANDED, 2 * NU - 1);
    }
    WAITV(0);
    ctl_wr(L, C_LANDED, 2 * NU + 1);
    STAMP(3); CYC(31); FLUSH();
    return;
  }

  const rsrc_t rws = mk(ws, W_END);
  const int n16 = lane & 15, g4 = lane >> 4;
  const int q4 = n16 >> 2, p4 = lane & 3;
  const int row = grp * 16 + j;
  u32x2 ep = {0u, 0u};
  const u32x4 srd = {(unsigned)(size_t)ws, (unsigned)((size_t)ws >> 32) & 0xFFFFu, (unsigned)W_END, 0x00020000u};
  if (wave == 3) {
    const rsrc_t rq = mk(wq, DM * 4096 * 2);
    const int p = bid >> 3;
#pragma nounroll
    for (int u = 0; u < 14; ++u)
      __builtin_amdgcn_raw_ptr_buffer_load_lds(rq, (LAS void*)(L + L_STG + u * 1024), 16, (lane >> 1) * 8192 + (lane & 1) * 16, u * 32 * 8192 + x8 * 1024 + 32 * p, 0, AUX_Q);
    ep = __builtin_amdgcn_raw_buffer_load_b64(rws, 0, W_EPOCH, 0);
    LAS char* wq_rd = pin(L, L_STG + (8 * g4 + q4) * 32 + 8 * p4);
    LAS char* xn_rd = pin(L, L_XN + (lane & 1) * 896 + g4 * 16);
    __builtin_amdgcn_s_barrier();
    WAITV(0);
    STAMP(4);
    u32x4 wf[14];
#pragma unroll
    for (int s = 0; s < 14; ++s) wf[s] = trread2(wq_rd + s * 1024, 128);
    ctl_wait(L, C_XN, 2);
    u32x4 af[14];
#pragma unroll
    for (int s = 0; s < 14; ++s) af[s] = *(LAS u32x4*)(xn_rd + s * 64);
    __builtin_amdgcn_sched_barrier(0);
    f32x4 d = {0.f, 0.f, 0.f, 0.f};
#pragma unroll
    for (int s = 0; s < 14; ++s) d = mfma32(af[s], wf[s], d);
    const float qv = (g4 & 1) ? d[1] : d[0];
    const unsigned pr = pack2(qv, dpp<XOR1>(qv));
    if (lane < 32 && !(lane & 1))
      __builtin_amdgcn_raw_buffer_store_b64(u32x2{pr, ep[0] + 1u}, rws, ((g4 * 8 + x8) * 256 + 8 * p + (n16 >> 1)) * 8, W_Q, AUX_SC1);
    STAMP(5);
  } else if (wave > 0) {
    const int bb = wave - 1;
    const u32x4 xv = *(const u32x4*)(x + bb * DM + (lane < 56 ? lane : 55) * 8);
    ep = __builtin_amdgcn_raw_buffer_load_b64(rws, 0, W_EPOCH, 0);
    __builtin_amdgcn_s_barrier();
    float v[8];
#pragma unroll
    for (int i = 0; i < 4; ++i) { v[2 * i] = bf_lo(xv[i]); v[2 * i + 1] = bf_hi(xv[i]); }
    const u32x4 o = layernorm8(v, lane < 56);
    if (lane < 56) *(LAS u32x4*)(L + L_XN + bb * 896 + lane * 16) = o;
    ctl_add(L, C_XN, lane);
  } else {
    ep = __builtin_amdgcn_raw_buffer_load_b64(rws, 0, W_EPOCH, 0);
    ((LAS int*)(L + L_CTL))[lane] = lane < 4 ? lane : 0;
    BARRIER();
    const u32x4 s4 = {SENT, SENT, SENT, SENT};
    __builtin_amdgcn_raw_buffer_store_b128(s4, rws, lane * 16, W_PART + row * 2048, AUX_SC1);
    __builtin_amdgcn_raw_buffer_store_b128(s4, rws, lane * 16 + 1024, W_PART + row * 2048, AUX_SC1);
    if (lane == 0) __builtin_amdgcn_raw_buffer_store_b128(s4, rws, 0, W_ML + row * 16, AUX_SC1);
    if (is_wo && lane < 32) __builtin_amdgcn_raw_buffer_store_b128(s4, rws, g4 * 16384 + n16 * 16, W_Y + x8 * 2048 + rk * 256, AUX_SC1);
    if (is_ffn) {
#pragma unroll
      for (int it = 0; it < 4; ++it) {
        const int r = (lane >> 2) + 16 * it;
        if (r < NFFN) __builtin_amdgcn_raw_buffer_store_b128(s4, rws, (r * NFFN + f) * 64 + p4 * 16, W_Z, AUX_SC1);
      }
    }
    WAITV(0);
    if (lane == 0) __builtin_amdgcn_raw_buffer_store_b64(u32x2{ep[0] + 1u, ep[1]}, rws, bid * 8, W_FLAG, AUX_SC1);
  }
  CFENCE();

  const int ka = (n16 >> 1) * KBLK + (n16 & 1) * 512 + (16 * (g4 & 1) + 8 * ((g4 >> 1) ^ (n16 & 1))) * 16;
  LAS char* sc_wr = L + L_STG + wave * 2048 + n16 * 32 + g4 * 8;
  int seen = 0;
  float mx = -INFINITY;
  u32x4 qf[8];
  auto score = [&](const u32x4 (&kf)[8], int i) {
    f32x4 s0 = {0.f, 0.f, 0.f, 0.f}, s1 = {0.f, 0.f, 0.f, 0.f};
#pragma unroll
    for (int s = 0; s < 8; s += 2) { s0 = mfma32(kf[s], qf[s], s0); s1 = mfma32(kf[s + 1], qf[s + 1], s1); }
    const u32x2 sb = {pack2(s0[0] + s1[0], s0[1] + s1[1]), pack2(s0[2] + s1[2], s0[3] + s1[3])};
    mx = fmaxf(fmaxf(mx, fmaxf(bf_lo(sb[0]), bf_hi(sb[0]))), fmaxf(bf_lo(sb[1]), bf_hi(sb[1])));
    if (n16 < 2) *(LAS u32x2*)(sc_wr + i * 64) = sb;
  };
  {
    u32x4 kp[RPRE][8];
#pragma unroll
    for (int i = 0; i < RPRE; ++i) {
      const int u = 4 * i + wave;
      landed_wait(L, seen, u + 1);
#pragma unroll
      for (int s = 0; s < 8; ++s) kp[i][s] = *(LAS u32x4*)(L + u * SLOT + ka + 16 * s);
      WAITL();
      ctl_wr(L, C_PROG + wave, u + 4);
    }
    if (wave == 1) {
      u32x4 g0, g1;
      const unsigned tg = ep[0] + 1u;
      int budget = SPIN_CAP;
      do {
        CFENCE();
        g0 = __builtin_amdgcn_raw_buffer_load_b128(rws, lane * 16, W_Q + grp * 2048, AUX_SC1);
        g1 = __builtin_amdgcn_raw_buffer_load_b128(rws, lane * 16 + 1024, W_Q + grp * 2048, AUX_SC1);
      } while (__any(((g0[1] ^ tg) | (g0[3] ^ tg) | (g1[1] ^ tg) | (g1[3] ^ tg)) != 0u) && --budget > 0);
      *(LAS u32x2*)(L + L_QB + lane * 8) = u32x2{g0[0], g0[2]};
      *(LAS u32x2*)(L + L_QB + 512 + lane * 8) = u32x2{g1[0], g1[2]};
      WAITL();
      ctl_wr(L, C_Q, 1);
      STAMP(6);
    } else {
      ctl_wait(L, C_Q, 1);
    }
#pragma unroll
    for (int s = 0; s < 8; ++s) qf[s] = *(LAS u32x4*)(L + L_QB + (lane & 1) * 512 + (s + 16 * (g4 & 1) + 8 * (g4 >> 1)) * 16);
#pragma unroll
    for (int i = 0; i < RPRE; ++i) score(kp[i], i);
  }
  for (int i = RPRE; i < NU / 4; ++i) {
    const int u = 4 * i + wave;
    landed_wait(L, seen, u + 1);
    u32x4 kf[8];
#pragma unroll
    for (int s = 0; s < 8; ++s) kf[s] = *(LAS u32x4*)(L + (u & (NS - 1)) * SLOT + ka + 16 * s);
    WAITL();
    ctl_wr(L, C_PROG + wave, min(u + 4, NU));
    score(kf, i);
  }

  mx = xrow_max(mx);
  if (lane < 2) *(LAS float*)(L + L_RED + wave * 8 + lane * 4) = mx;
  ctl_add(L, C_MX, lane);
  ctl_wait(L, C_MX, 4);
  {
    const LAS float* r = (const LAS float*)(L + L_RED + (lane & 1) * 4);
    const float M = fmaxf(fmaxf(r[0], r[2]), fmaxf(r[4], r[6]));
    LAS u32x4* sp = (LAS u32x4*)(L + L_STG + wave * 2048 + lane * 32);
    float ls = 0.f;
#pragma unroll
    for (int hh = 0; hh < 2; ++hh) {
      u32x4 v = sp[hh];
#pragma unroll
      for (int i = 0; i < 4; ++i) {
        const float p0 = __builtin_amdgcn_exp2f((bf_lo(v[i]) - M) * C2), p1 = __builtin_amdgcn_exp2f((bf_hi(v[i]) - M) * C2);
        ls += p0 + p1; v[i] = pack2(p0, p1);
      }
      sp[hh] = v;
    }
    ls += dpp<XOR2>(ls); ls += dpp<ROR4>(ls); ls += dpp<ROR8>(ls); ls = xrow_sum(ls);
    if (lane < 2) *(LAS float*)(L + L_RED + 32 + wave * 8 + lane * 4) = ls;
    ctl_add(L, C_P, lane);
    ctl_wait(L, C_P, 4);
    if (wave == 0) {
      if (lane < 2) {
        const float Ls = (r[8] + r[10]) + (r[12] + r[14]);
        __builtin_amdgcn_raw_buffer_store_b64(u32x2{pub(M), pub(Ls)}, rws, lane * 8, W_ML + row * 16, AUX_SC1);
      }
      STAMP(15);
    }
  }

  f32x4 acc[4];
#pragma unroll
  for (int c = 0; c < 4; ++c) acc[c] = f32x4{0.f, 0.f, 0.f, 0.f};
  {
    const int va = (2 * g4 + (q4 >> 1)) * VBLK + (q4 & 1) * 512 + (p4 >> 1) * 16 + (p4 & 1) * 8 + ((wave & 1) ? -128 : 128) * (q4 & 1) + 128 * wave;
    LAS char* p_rd = L + L_STG + (lane & 1) * 32 + g4 * 8;
    for (int k = 0; k < NU; ++k) {
      const u32x2 pb = *(LAS u32x2*)(p_rd + (k & 3) * 2048 + (k >> 2) * 64);
      landed_wait(L, seen, NU + k + 1);
      LAS char* sl = L + (k & (NS - 1)) * SLOT + va;
      u32x2 vf[4];
#pragma unroll
      for (int c = 0; c < 4; ++c) vf[c] = trread(sl + 32 * c);
      WAITL();
      ctl_wr(L, C_PROG + wave, NU + k + 1);
#pragma unroll
      for (int c = 0; c < 4; ++c) acc[c] = mfma16(vf[c], pb, acc[c]);
      if (k == 8 && wave == 0 && (is_wo || is_ffn || bid == 0)) {
        for (int s = 0; s < SPIN_CAP; ++s) {
          const u32x4 a = __builtin_amdgcn_raw_buffer_load_b128(rws, lane * 16, W_FLAG, AUX_SC1);
          const u32x4 c = __builtin_amdgcn_raw_buffer_load_b128(rws, lane * 16 + 1024, W_FLAG, AUX_SC1);
          const unsigned lo = ep[0] + 1u, hi = ep[1];
          if (__all(a[0] == lo && a[1] == hi && a[2] == lo && a[3] == hi && c[0] == lo && c[1] == hi && c[2] == lo && c[3] == hi)) break;
          __builtin_amdgcn_s_sleep(1);
          CFENCE();
        }
        ctl_wr(L, C_VER, 1);
        if (bid == 0 && lane == 0) __builtin_amdgcn_raw_buffer_store_b64(u32x2{ep[0] + 1u, ep[1]}, rws, 0, W_EPOCH, AUX_SC1);
      }
    }
  }
  if (wave == 3) STAMP(7);
  {
    const f32x4 a = (n16 & 2) ? acc[1] : acc[0], c = (n16 & 2) ? acc[3] : acc[2], o = (n16 & 4) ? c : a;
    if (n16 < 8)
      __builtin_amdgcn_raw_buffer_store_b128(u32x4{pub(o[0]), pub(o[1]), pub(o[2]), pub(o[3])}, rws,
                                             wave * 512 + (n16 & 1) * 256 + (n16 >> 1) * 64 + g4 * 16, W_PART + row * 2048, AUX_SC1);
  }
  if (wave == 3) STAMP(8);

  if (is_wo) {
    const int hf = lane >> 5, gsel = hf * 8 + x8;
    ctl_wait(L, C_VER, 1);
    u32x2 ml;
    for (int s = 0; s < SPIN_CAP; ++s) {
      ml = __builtin_amdgcn_raw_buffer_load_b64(rws, gsel * 256 + n16 * 16 + (g4 & 1) * 8, W_ML, AUX_SC1);
      if (__all(max(ml[0], ml[1]) != SENT)) break;
      __builtin_amdgcn_s_sleep(4);
      CFENCE();
    }
    const float mj = __uint_as_float(ml[0]);
    float wt = __builtin_amdgcn_exp2f((mj - row_max(mj)) * C2);
    wt *= 1.f / row_sum(wt * __uint_as_float(ml[1]));
    landed_wait(L, seen, 2 * NU + 1);
    u32x4 wf[16];
#pragma unroll
    for (int s = 0; s < 16; ++s) wf[s] = trread2(L + (32 * s + 8 * g4 + q4) * 128 + wave * 32 + 8 * p4, 512);
    if (wave == 0) STAMP(16);
    for (int s = 0; s < SPIN_CAP; ++s) {
      const unsigned probe = __builtin_amdgcn_raw_buffer_load_b32(rws, gsel * 32768 + n16 * 2048 + wave * 512 + (g4 & 1) * 256 + 252, W_PART, AUX_SC1);
      if (__builtin_popcountll(__ballot(probe == SENT)) <= 16) break;
      __builtin_amdgcn_s_sleep(2);
      CFENCE();
    }
    float wb[16];
#define BC_(k) wb[k] = dpp<NEWBC + k>(wt);
    BC_(0) BC_(1) BC_(2) BC_(3) BC_(4) BC_(5) BC_(6) BC_(7) BC_(8) BC_(9) BC_(10) BC_(11) BC_(12) BC_(13) BC_(14) BC_(15)
#undef BC_
    LAS char* ob_wr = pin(L, L_OB + hf * 1024 + (g4 & 1) * 512 + wave * 128 + n16 * 8);
    LAS char* ob_rd = pin(L, L_OB + (lane & 1) * 1024 + g4 * 16);
    int y_off = g4 * 16384 + wave * 64 + n16 * 4;
    asm volatile("" : "+v"(y_off));
    u32x4 pv[16];
#pragma unroll
    for (int k = 0; k < 16; ++k) pv[k] = u32x4{0u, 0u, 0u, SENT};
    f32x4 o0, o1;
    const int pvo = gsel * 32768 + wave * 512 + (lane & 31) * 16;
    int budget = SPIN_CAP;
    bool again;
    do {
      do {
#pragma unroll
        for (int k = 0; k < 16; k += 4) reload4(pv[k], pv[k + 1], pv[k + 2], pv[k + 3], pvo, srd, W_PART + k * 2048, W_PART + k * 2048 + 4096);
        LANDED8(pv, 0); LANDED8(pv, 8);
      } while (pending16(pv) && --budget > 0);
      o0 = wb[0] * __builtin_bit_cast(f32x4, pv[0]); o1 = wb[1] * __builtin_bit_cast(f32x4, pv[1]);
#pragma unroll
      for (int k = 2; k < 16; k += 2) { o0 += wb[k] * __builtin_bit_cast(f32x4, pv[k]); o1 += wb[k + 1] * __builtin_bit_cast(f32x4, pv[k + 1]); }
      o0 += o1;
      const float chk = (o0[0] + o0[1]) + (o0[2] + o0[3]);
      again = false;
      if (__builtin_expect(__any(chk != chk), 0)) again = rearm16(pv) && --budget > 0;
    } while (__builtin_expect(again, 0));
    if (wave == 0) STAMP(9);
    *(LAS u32x2*)ob_wr = u32x2{pack2(o0[0], o0[1]), pack2(o0[2], o0[3])};
    BARRIER();
    f32x4 d0 = {0.f, 0.f, 0.f, 0.f}, d1 = d0;
    u32x4 af[16];
#pragma unroll
    for (int s = 0; s < 16; ++s) af[s] = *(LAS u32x4*)(ob_rd + s * 64);
    __builtin_amdgcn_sched_barrier(0);
#pragma unroll
    for (int s = 0; s < 16; s += 2) { d0 = mfma32(af[s], wf[s], d0); d1 = mfma32(af[s + 1], wf[s + 1], d1); }
    if (lane < 32)
      __builtin_amdgcn_raw_buffer_store_b32(pub((g4 & 1) ? d0[1] + d1[1] : d0[0] + d1[0]), rws, y_off, W_Y + x8 * 2048 + rk * 256, AUX_SC1);
    if (wave == 0) STAMP(10);
  }
  if (!is_ffn) { FLUSH(); return; }

  const int ln = lane < 56 ? lane : 55;
  u32x2 xa = {0u, 0u}, xb = {0u, 0u};
  if (wave < 2) { xa = *(const u32x2*)(x + wave * DM + ln * 4); xb = *(const u32x2*)(x + wave * DM + 224 + ln * 4); }
  ctl_wait(L, C_VER, 1);
  landed_wait(L, seen, 2 * NU + 1);
  u32x4 gf[14], df[7];
#pragma unroll
  for (int s = 0; s < 14; ++s) gf[s] = trread2(L + (p4 >> 1) * 28672 + (32 * s + 8 * g4 + q4) * 64 + wave * 16 + 8 * (p4 & 1), 256);
#pragma unroll
  for (int i = 0; i < 7; ++i) df[i] = trread2(L + 57344 + (8 * g4 + q4) * 896 + (7 * wave + i) * 32 + 8 * p4, 4 * 896);
  LAS char* xn_rd = pin(L, L_XN + (lane & 1) * 896 + g4 * 16);
  LAS char* xn_wr = pin(L, L_XN + wave * 896 + lane * 8);
  LAS char* hb_wr = pin(L, L_HB + wave * 1792 + lane * 16);
  LAS char* gb_wr = pin(L, L_GB + g4 * 64 + (8 * wave + n16) * 2);
  LAS char* gb_rd = pin(L, L_GB + (lane & 1) * 64 + g4 * 16);
  int z_off = (((g4 & 1) * 28 + 7 * wave + (g4 >> 1)) * NFFN + f) * 64 + n16 * 4;
  asm volatile("" : "+v"(z_off));
  if (wave == 0) STAMP(17);
  if (wave < 2) {
    for (int s = 0; s < SPIN_CAP; ++s) {
      const unsigned probe = __builtin_amdgcn_raw_buffer_load_b32(rws, (ln / 7) * 2048 + (ln % 7) * 256 + 252, W_Y + wave * 16384, AUX_SC1);
      if (__builtin_popcountll(__ballot(probe == SENT)) <= 21) break;
      __builtin_amdgcn_s_sleep(2);
      CFENCE();
    }
    u32x4 yv[16];
#pragma unroll
    for (int k = 0; k < 16; ++k) yv[k] = u32x4{0u, 0u, 0u, SENT};
    f32x4 sa, sb;
    int budget = SPIN_CAP;
    bool again;
    do {
      do {
#pragma unroll
        for (int k = 0; k < 8; k += 4) {
          reload4(yv[k], yv[k + 1], yv[k + 2], yv[k + 3], ln * 16, srd, W_Y + wave * 16384 + k * 2048, W_Y + wave * 16384 + k * 2048 + 4096);
          reload4(yv[8 + k], yv[9 + k], yv[10 + k], yv[11 + k], ln * 16 + 896, srd, W_Y + wave * 16384 + k * 2048, W_Y + wave * 16384 + k * 2048 + 4096);
        }
        LANDED8(yv, 0); LANDED8(yv, 8);
      } while (pending16(yv) && --budget > 0);
      sa = __builtin_bit_cast(f32x4, yv[0]); sb = __builtin_bit_cast(f32x4, yv[8]);
#pragma unroll
      for (int k = 1; k < 8; ++k) { sa += __builtin_bit_cast(f32x4, yv[k]); sb += __builtin_bit_cast(f32x4, yv[8 + k]); }
      const f32x4 t = sa + sb;
      const float chk = (t[0] + t[1]) + (t[2] + t[3]);
      again = false;
      if (__builtin_expect(__any(chk != chk), 0)) again = rearm16(yv) && --budget > 0;
    } while (__builtin_expect(again, 0));
    if (wave == 0) STAMP(11);
    float v[8];
#pragma unroll
    for (int i = 0; i < 2; ++i) {
      const unsigned ra = pack2(sa[2 * i], sa[2 * i + 1]), rb = pack2(sb[2 * i], sb[2 * i + 1]);
      const unsigned ha = pack2(bf_lo(xa[i]) + bf_lo(ra), bf_hi(xa[i]) + bf_hi(ra)), hb = pack2(bf_lo(xb[i]) + bf_lo(rb), bf_hi(xb[i]) + bf_hi(rb));
      v[2 * i] = bf_lo(ha); v[2 * i + 1] = bf_hi(ha); v[4 + 2 * i] = bf_lo(hb); v[5 + 2 * i] = bf_hi(hb);
    }
    const u32x4 o = layernorm8(v, lane < 56);
    if (lane < 56) {
      *(LAS u32x2*)xn_wr = u32x2{o[0], o[1]};
      *(LAS u32x2*)(xn_wr + 448) = u32x2{o[2], o[3]};
      *(LAS f32x4*)hb_wr = f32x4{v[0], v[1], v[2], v[3]};
      *(LAS f32x4*)(hb_wr + 896) = f32x4{v[4], v[5], v[6], v[7]};
    }
  }
  BARRIER();
  {
    f32x4 d0 = {0.f, 0.f, 0.f, 0.f}, d1 = d0;
    u32x4 af[14];
#pragma unroll
    for (int s = 0; s < 14; ++s) af[s] = *(LAS u32x4*)(xn_rd + s * 64);
    __builtin_amdgcn_sched_barrier(0);
#pragma unroll
    for (int s = 0; s < 14; s += 2) { d0 = mfma32(af[s], gf[s], d0); d1 = mfma32(af[s + 1], gf[s + 1], d1); }
    const float a = rbf((g4 & 1) ? d0[1] + d1[1] : d0[0] + d1[0]), u = dpp<ROR8>(a);
    const float z = 0.7978845608028654f * (a + 0.044715f * a * a * a);
    const float ge = rbf(a * __builtin_amdgcn_rcpf(1.f + __builtin_amdgcn_exp2f(-2.f * LOG2E * z)));
    if (lane < 32 && n16 < 8) *(LAS u16*)gb_wr = (u16)pack2(ge * u, 0.f);
  }
  BARRIER();
  {
    const u32x4 a = *(LAS u32x4*)gb_rd;
    f32x4 d[8];
#pragma unroll
    for (int i = 0; i < 7; ++i) d[i] = mfma32(a, df[i], f32x4{0.f, 0.f, 0.f, 0.f});
    d[7] = d[6];
#pragma unroll
    for (int i = 0; i < 4; ++i) {
      const float v0 = (g4 & 1) ? d[2 * i][1] : d[2 * i][0], v1 = (g4 & 1) ? d[2 * i + 1][1] : d[2 * i + 1][0];
      if (i < 3 || lane < 32) __builtin_amdgcn_raw_buffer_store_b32(pub((g4 >> 1) ? v1 : v0), rws, z_off, W_Z + 2 * i * NFFN * 64, AUX_SC1);
    }
  }
  if (wave != 0) { FLUSH(); return; }
  STAMP(12);

  {
    const int ob = f / 28, oc = (f % 28) * 16 + p4 * 4;
    const f32x4 hv = *(LAS f32x4*)(L + L_HB + ob * 1792 + oc * 4);
    u32x4 zv[4];
    int budget = SPIN_CAP;
    unsigned mxx;
    do {
      CFENCE();
#pragma unroll
      for (int k = 0; k < 4; ++k) zv[k] = __builtin_amdgcn_raw_buffer_load_b128(rws, (k < 3 ? lane : (lane & 31)) * 16 + k * 1024, W_Z + f * (NFFN * 64), AUX_SC1);
      mxx = max(max(umax4(zv[0]), umax4(zv[1])), max(umax4(zv[2]), umax4(zv[3])));
    } while (__any(mxx == SENT) && --budget > 0);
    STAMP(13);
    f32x4 sum = (__builtin_bit_cast(f32x4, zv[0]) + __builtin_bit_cast(f32x4, zv[1])) + __builtin_bit_cast(f32x4, zv[2]);
    if (lane < 32) sum += __builtin_bit_cast(f32x4, zv[3]);
    float r[4];
#pragma unroll
    for (int i = 0; i < 4; ++i) { float t = sum[i]; t += dpp<ROR4>(t); t += dpp<ROR8>(t); r[i] = hv[i] + rbf(xrow_sum(t)); }
    if (lane < 4) *(u32x2*)(out + ob * DM + oc) = u32x2{pack2(r[0], r[1]), pack2(r[2], r[3])};
    STAMP(14); FLUSH();
  }
}

extern "C" void kernel_launch(void* const* d_in, const int* in_sizes, int n_in, void* d_out, int out_size, void* d_ws, size_t ws_size,
                              hipStream_t stream) {
  fused_decode<<<256, 320, 0, stream>>>((const u16*)d_in[0], (const u16*)d_in[1], (const u16*)d_in[2], (const u16*)d_in[3], (const u16*)d_in[4],
                                        (const u16*)d_in[6], (const u16*)d_in[7], (const u16*)d_in[8], (u16*)d_out, (char*)d_ws);
}
```

```cpp
#include <hip/hip_runtime.h>
#include <hip/hip_bf16.h>
#include <stdint.h>

#define LAS __attribute__((address_space(3)))
typedef unsigned short u16;
typedef unsigned long long u64;
typedef unsigned u32x2 __attribute__((ext_vector_type(2)));
typedef unsigned u32x4 __attribute__((ext_vector_type(4)));
typedef float f32x2 __attribute__((ext_vector_type(2)));
typedef float f32x4 __attribute__((ext_vector_type(4)));
typedef short s16x4 __attribute__((ext_vector_type(4)));
typedef __bf16 bf16x2 __attribute__((ext_vector_type(2)));
typedef __bf16 bf16x8 __attribute__((ext_vector_type(8)));
typedef __amdgpu_buffer_rsrc_t rsrc_t;

#define NFL 20
#define NFLW 40
#define AUX_W 2
#define AUX_Q 2
#define AUX_K 2
#define AUX_V 2
#define NFLV NFL
#define GSPLIT 9
#define NHIT 20
#define NMISS 24
#define KSEL false
#define VSEL false
#define KHIT 20
#define KMISS 20
#define GM 0
#define RPRE 3

constexpr int DM = 448, SK = 32768, DH = 256, FF = 1792;
constexpr int NU = 128;
constexpr int NS = 16, KBLK = 1040, VBLK = 1056, SLOT = 8 * VBLK;
constexpr int L_STG = NS * SLOT, L_XN = L_STG + 14336, L_QB = L_XN + 1792, L_HB = L_QB + 1024, L_GB = L_HB + 3584,
              L_OB = L_GB + 128, L_RED = L_OB + 2048, L_CTL = L_RED + 64, L_STAMP = L_CTL + 256, L_END = L_STAMP + 1280;
constexpr int C_PROG = 0, C_LANDED = 4, C_XN = 5, C_Q = 6, C_VER = 7, C_MX = 8, C_P = 9;
constexpr int W_EPOCH = 0, W_FLAG = 4096, W_Q = 8192, W_ML = 40960, W_Y = 49152, W_PART = 131072, W_Z = 655360,
              W_END = 1 << 20;
constexpr int WO0 = 144, FFN0 = 200, NFFN = 56;
constexpr unsigned SENT = 0xFFFFFFFFu;
constexpr int AUX_SC1 = 16, AUX_NT = 2;
constexpr int SPIN_CAP = 1 << 20;
constexpr float LOG2E = 1.4426950408889634f, C2 = 0.0625f * LOG2E;

#define WAITV(n) asm volatile("s_waitcnt vmcnt(%0)" :: "i"(n) : "memory")
#define WAITL() asm volatile("s_waitcnt lgkmcnt(0)" ::: "memory")
#define CFENCE() asm volatile("" ::: "memory")
#define BARRIER() do { WAITL(); __builtin_amdgcn_s_barrier(); CFENCE(); } while (0)

#define STAMP(k) do { } while (0)
#define CYC(k) do { } while (0)
#define FLUSH() do { } while (0)

__device__ __forceinline__ float bf_lo(unsigned v) { return __uint_as_float(v << 16); }
__device__ __forceinline__ float bf_hi(unsigned v) { return __uint_as_float(v & 0xffff0000u); }
__device__ __forceinline__ unsigned pack2(float lo, float hi) { return __builtin_bit_cast(unsigned, __builtin_convertvector(f32x2{lo, hi}, bf16x2)); }
__device__ __forceinline__ float rbf(float f) { return bf_lo(pack2(f, f)); }
__device__ __forceinline__ unsigned pub(float f) { unsigned u = __float_as_uint(f); return u == SENT ? 0x7FC00000u : u; }
template <int CTRL> __device__ __forceinline__ float dpp(float x) {
  return __builtin_bit_cast(float, __builtin_amdgcn_mov_dpp(__builtin_bit_cast(int, x), CTRL, 0xf, 0xf, true));
}
constexpr int XOR1 = 0xB1, XOR2 = 0x4E, XOR7 = 0x141, ROR8 = 0x128, ROR4 = 0x124, NEWBC = 0x150;
__device__ __forceinline__ float xrow_max(float x) {
  auto s = __builtin_amdgcn_permlane16_swap(__float_as_uint(x), __float_as_uint(x), false, false);
  x = fmaxf(__uint_as_float(s[0]), __uint_as_float(s[1]));
  auto t = __builtin_amdgcn_permlane32_swap(__float_as_uint(x), __float_as_uint(x), false, false);
  return fmaxf(__uint_as_float(t[0]), __uint_as_float(t[1]));
}
__device__ __forceinline__ float xrow_sum(float x) {
  auto s = __builtin_amdgcn_permlane16_swap(__float_as_uint(x), __float_as_uint(x), false, false);
  x = __uint_as_float(s[0]) + __uint_as_float(s[1]);
  auto t = __builtin_amdgcn_permlane32_swap(__float_as_uint(x), __float_as_uint(x), false, false);
  return __uint_as_float(t[0]) + __uint_as_float(t[1]);
}
__device__ __forceinline__ float row_sum(float x) { x += dpp<XOR1>(x); x += dpp<XOR2>(x); x += dpp<XOR7>(x); x += dpp<ROR8>(x); return x; }
__device__ __forceinline__ float row_max(float x) {
  x = fmaxf(x, dpp<XOR1>(x)); x = fmaxf(x, dpp<XOR2>(x)); x = fmaxf(x, dpp<XOR7>(x)); x = fmaxf(x, dpp<ROR8>(x)); return x;
}
__device__ __forceinline__ float wave_sum(float x) { return xrow_sum(row_sum(x)); }
__device__ __forceinline__ unsigned umax4(u32x4 v) { return max(max(v[0], v[1]), max(v[2], v[3])); }

__device__ __forceinline__ rsrc_t mk(const void* p, unsigned bytes) { return __builtin_amdgcn_make_buffer_rsrc((void*)p, 0, bytes, 0x00020000); }
__device__ __forceinline__ f32x4 mfma32(u32x4 a, u32x4 b, f32x4 c) {
  return __builtin_amdgcn_mfma_f32_16x16x32_bf16(__builtin_bit_cast(bf16x8, a), __builtin_bit_cast(bf16x8, b), c, 0, 0, 0);
}
__device__ __forceinline__ f32x4 mfma16(u32x2 a, u32x2 b, f32x4 c) {
  return __builtin_amdgcn_mfma_f32_16x16x16bf16_1k(__builtin_bit_cast(s16x4, a), __builtin_bit_cast(s16x4, b), c, 0, 0, 0);
}
__device__ __forceinline__ u32x2 trread(LAS char* p) {
  return __builtin_bit_cast(u32x2, __builtin_amdgcn_ds_read_tr16_b64_v4i16((LAS s16x4*)p));
}
__device__ __forceinline__ u32x4 trread2(LAS char* p, int stride4) {
  const u32x2 a = trread(p), b = trread(p + stride4);
  return u32x4{a[0], a[1], b[0], b[1]};
}
__device__ __forceinline__ LAS char* pin(LAS char* L, int off) { asm volatile("" : "+v"(off)); return L + off; }
__device__ __forceinline__ int ctl_rd(LAS char* L, int w) { return __builtin_amdgcn_readfirstlane(*(volatile LAS int*)(L + L_CTL + 4 * w)); }
__device__ __forceinline__ void ctl_wr(LAS char* L, int w, int v) { *(volatile LAS int*)(L + L_CTL + 4 * w) = v; }
__device__ __forceinline__ void ctl_wait(LAS char* L, int w, int need) {
  int s = SPIN_CAP;
  while (ctl_rd(L, w) < need && --s > 0) __builtin_amdgcn_s_sleep(1);
  CFENCE();
}
__device__ __forceinline__ void landed_wait(LAS char* L, int& seen, int need) {
  if (seen < need) {
    int s = SPIN_CAP;
    do { seen = ctl_rd(L, C_LANDED); } while (seen < need && --s > 0);
  }
  CFENCE();
}
__device__ __forceinline__ void ctl_add(LAS char* L, int w, int lane) {
  WAITL();
  if (lane == 0) __hip_atomic_fetch_add((LAS int*)(L + L_CTL + 4 * w), 1, __ATOMIC_RELAXED, __HIP_MEMORY_SCOPE_WORKGROUP);
  CFENCE();
}
__device__ __forceinline__ int prog_min(LAS char* L) {
  const u32x4 d = *(volatile LAS u32x4*)(L + L_CTL + 4 * C_PROG);
  return (int)min(min(d[0], d[1]), min(d[2], d[3]));
}
__device__ __forceinline__ void wait_released(LAS char* L, int unit) {
  int s = SPIN_CAP, v;
  do { v = __builtin_amdgcn_readfirstlane(prog_min(L)); } while (v <= unit && --s > 0);
  CFENCE();
}

__device__ __forceinline__ u32x4 layernorm8(const float (&v)[8], bool act) {
  const float c = __builtin_bit_cast(float, __builtin_amdgcn_readfirstlane(__builtin_bit_cast(int, v[0])));
  float s = 0.f, ss = 0.f;
#pragma unroll
  for (int i = 0; i < 8; ++i) { const float d = v[i] - c; s += d; ss = fmaf(d, d, ss); }
  s = wave_sum(act ? s : 0.f) * (1.f / DM);
  ss = wave_sum(act ? ss : 0.f) * (1.f / DM);
  const float var = rbf(fmaxf(fmaf(-s, s, ss), 0.f));
  const float inv = __builtin_amdgcn_rcpf(rbf(__builtin_amdgcn_sqrtf(rbf(var + 1e-5f))));
  const float mb = rbf(c + s);
  u32x4 o;
#pragma unroll
  for (int i = 0; i < 4; ++i) { const unsigned d = pack2(v[2 * i] - mb, v[2 * i + 1] - mb); o[i] = pack2(bf_lo(d) * inv, bf_hi(d) * inv); }
  return o;
}

template <int BLK, int AUX, int NW>
__device__ __forceinline__ void load_units(LAS char* L, rsrc_t r, int vo, int so0, int u0, int& published, int& fr, int k0 = 0, int k1 = NU) {
  for (int k = k0; k < k1; ++k) {
    const int u = u0 + k;
    LAS char* sl = L + (u & (NS - 1)) * SLOT;
    const int so = so0 + (k >> 1) * 32768 + (k & 1) * 8192;
    if (u >= NS && __builtin_amdgcn_readfirstlane(fr) <= u - NS) {
      WAITV(0);
      published = u; ctl_wr(L, C_LANDED, u);
      wait_released(L, u - NS);
    }
#pragma unroll
    for (int i = 0; i < 8; ++i) {
      __builtin_amdgcn_raw_ptr_buffer_load_lds(r, (LAS void*)(sl + i * BLK), 16, vo, so + i * 1024, 0, AUX);
      WAITV(NW - 1);
      if ((i + 2 - NW + 64) % 8 == 0) {
        const int n = u + (i + 2 - NW + 64) / 8 - 8;
        if (n > published) { published = n; ctl_wr(L, C_LANDED, n); }
      }
      if (i == 4) fr = prog_min(L);
    }
    if (u == 1) { __builtin_amdgcn_s_barrier(); CFENCE(); }
  }
}
__device__ __forceinline__ void load_weight_head(LAS char* L, rsrc_t r, int vo, int so, int step) {
#pragma unroll
  for (int k = 0; k < 28; ++k) {
    __builtin_amdgcn_raw_ptr_buffer_load_lds(r, (LAS void*)(L + k * 1024), 16, vo, so + k * step, 0, AUX_W);
    if (k <= NFL - 2) {
      WAITV(NFL - 1);
      if ((k + 2 - NFL + 64) % 8 == 0) ctl_wr(L, C_LANDED, 2 * NU + (k + 2 - NFL + 64) / 8 - 8);
    }
  }
}
__device__ __forceinline__ void load_weight_rest(LAS char* L, rsrc_t r, int vo, int so, int step, int dst, int n4) {
#pragma nounroll
  for (int k = 0; k < n4; ++k) {
#pragma unroll
    for (int i = 0; i < 4; ++i) __builtin_amdgcn_raw_ptr_buffer_load_lds(r, (LAS void*)(L + dst + (4 * k + i) * 1024), 16, vo, so + (4 * k + i) * step, 0, AUX_W);
    WAITV(NFLW - 4);
  }
}
__device__ __forceinline__ void reload4(u32x4& a, u32x4& b, u32x4& c, u32x4& d, int vo, u32x4 srd, int so01, int so23) {
  const u64 ma = __ballot(a[3] == SENT), mb = __ballot(b[3] == SENT), mc = __ballot(c[3] == SENT), md = __ballot(d[3] == SENT);
  asm volatile("s_mov_b64 exec, %4\n\tbuffer_load_dwordx4 %0, %8, %9, %10 offen sc1\n\t"
               "s_mov_b64 exec, %5\n\tbuffer_load_dwordx4 %1, %8, %9, %10 offen offset:2048 sc1\n\t"
               "s_mov_b64 exec, %6\n\tbuffer_load_dwordx4 %2, %8, %9, %11 offen sc1\n\t"
               "s_mov_b64 exec, %7\n\tbuffer_load_dwordx4 %3, %8, %9, %11 offen offset:2048 sc1\n\t"
               "s_mov_b64 exec, -1"
               : "+v"(a), "+v"(b), "+v"(c), "+v"(d)
               : "s"(ma), "s"(mb), "s"(mc), "s"(md), "v"(vo), "s"(srd), "s"(so01), "s"(so23)
               : "memory");
}
#define LANDED8(v, k) asm volatile("s_waitcnt vmcnt(0)" : "+v"(v[k]), "+v"(v[k + 1]), "+v"(v[k + 2]), "+v"(v[k + 3]), "+v"(v[k + 4]), "+v"(v[k + 5]), "+v"(v[k + 6]), "+v"(v[k + 7]) :: "memory")
__device__ __forceinline__ bool pending16(const u32x4 (&v)[16]) {
  unsigned m = v[0][3];
#pragma unroll
  for (int k = 1; k < 16; ++k) m = max(m, v[k][3]);
  return __any(m == SENT);
}
__device__ __forceinline__ bool rearm16(u32x4 (&v)[16]) {
  unsigned m = 0u;
#pragma unroll
  for (int k = 0; k < 16; ++k) m = max(m, umax4(v[k]));
  if (!__any(m == SENT)) return false;
#pragma unroll
  for (int k = 0; k < 16; ++k) v[k][3] = SENT;
  return true;
}

__global__ void __launch_bounds__(320) fused_decode(const u16* __restrict__ x, const u16* __restrict__ Kc, const u16* __restrict__ Vc,
                                                    const u16* __restrict__ wq, const u16* __restrict__ wo, const u16* __restrict__ wg,
                                                    const u16* __restrict__ wu, const u16* __restrict__ wd, u16* __restrict__ out, char* ws) {
  __shared__ __attribute__((aligned(16))) char lds[L_END];
  LAS char* L = (LAS char*)lds;
  const int lane = threadIdx.x & 63, wave = __builtin_amdgcn_readfirstlane(threadIdx.x >> 6);
  const int bid = blockIdx.x;
  const int grp = GM ? (bid & 15) : (bid >> 4), j = GM ? (bid >> 4) : (bid & 15);
  const bool is_ffn = bid >= FFN0, is_wo = bid >= WO0 && !is_ffn;
  const int x8 = bid & 7, rk = (bid - (is_ffn ? FFN0 : WO0)) >> 3, f = 7 * x8 + rk;

  if (wave == 4) {
    __builtin_amdgcn_s_setprio(3);
    const int vo = (lane >> 5) * 512 + (((lane & 31) ^ ((lane >> 5) << 3)) << 4);
    const int so0 = (j >> 1) * (2 << 20) + (j & 1) * 16384;
    int published = 0, fr = 0;
    {
      const rsrc_t rk_ = mk(Kc + (size_t)grp * SK * DH, SK * DH * 2);
      if (KSEL) load_units<KBLK, 0, KHIT>(L, rk_, vo, so0, 0, published, fr);
      else load_units<KBLK, 2, KMISS>(L, rk_, vo, so0, 0, published, fr);
    }
    STAMP(1);
    {
      const rsrc_t rv = mk(Vc + (size_t)grp * SK * DH, SK * DH * 2);
      if (VSEL) load_units<VBLK, 0, NHIT>(L, rv, vo, so0, NU, published, fr, 0, NU - 4);
      else load_units<VBLK, 2, NMISS>(L, rv, vo, so0, NU, published, fr, 0, NU - 4);
      if (VSEL) load_units<VBLK, 0, NFL>(L, rv, vo, so0, NU, published, fr, NU - 4, NU);
      else load_units<VBLK, 2, NFL>(L, rv, vo, so0, NU, published, fr, NU - 4, NU);
    }
    STAMP(2);
    if (is_wo || is_ffn) {
      const rsrc_t r0 = mk(is_wo ? wo : wg, is_wo ? 4096 * DM * 2 : DM * FF * 2);
      const int vo0 = is_wo ? (lane >> 3) * (DM * 2) + (lane & 7) * 16 : (lane >> 2) * (FF * 2) + (lane & 3) * 16;
      const int so0w = is_wo ? x8 * 512 * (DM * 2) + 128 * rk : 64 * f, step0 = is_wo ? 8 * DM * 2 : 16 * FF * 2;
      if (__builtin_amdgcn_readfirstlane(fr) <= 2 * NU - NS + 3) wait_released(L, 2 * NU - NS + 3);
      load_weight_head(L, r0, vo0, so0w, step0);
      wait_released(L, 2 * NU - NS + 10);
      if (is_wo) {
        load_weight_rest(L, r0, vo0, so0w + 28 * step0, step0, 28672, 9);
      } else {
        load_weight_rest(L, mk(wu, DM * FF * 2), vo0, so0w, step0, 28672, 7);
        load_weight_rest(L, mk(wd, FF * DM * 2), lane * 16, 32 * f * (DM * 2), 1024, 57344, 7);
      }
    } else {
      WAITV(16); ctl_wr(L, C_LANDED, 2 * NU - 2);
      WAITV(8); ctl_wr(L, C_LANDED, 2 * NU - 1);
    }
    WAITV(0);
    ctl_wr(L, C_LANDED, 2 * NU + 1);
    STAMP(3); CYC(31); FLUSH();
    return;
  }

  const rsrc_t rws = mk(ws, W_END);
  const int n16 = lane & 15, g4 = lane >> 4;
  const int q4 = n16 >> 2, p4 = lane & 3;
  const int row = grp * 16 + j;
  u32x2 ep = {0u, 0u};
  const u32x4 srd = {(unsigned)(size_t)ws, (unsigned)((size_t)ws >> 32) & 0xFFFFu, (unsigned)W_END, 0x00020000u};
  if (wave == 3) {
    const rsrc_t rq = mk(wq, DM * 4096 * 2);
    const int p = bid >> 3;
#pragma nounroll
    for (int u = 0; u < 14; ++u)
      __builtin_amdgcn_raw_ptr_buffer_load_lds(rq, (LAS void*)(L + L_STG + u * 1024), 16, (lane >> 1) * 8192 + (lane & 1) * 16, u * 32 * 8192 + x8 * 1024 + 32 * p, 0, AUX_Q);
    ep = __builtin_amdgcn_raw_buffer_load_b64(rws, 0, W_EPOCH, 0);
    LAS char* wq_rd = pin(L, L_STG + (8 * g4 + q4) * 32 + 8 * p4);
    LAS char* xn_rd = pin(L, L_XN + (lane & 1) * 896 + g4 * 16);
    __builtin_amdgcn_s_barrier();
    WAITV(0);
    STAMP(4);
    u32x4 wf[14];
#pragma unroll
    for (int s = 0; s < 14; ++s) wf[s] = trread2(wq_rd + s * 1024, 128);
    ctl_wait(L, C_XN, 2);
    u32x4 af[14];
#pragma unroll
    for (int s = 0; s < 14; ++s) af[s] = *(LAS u32x4*)(xn_rd + s * 64);
    __builtin_amdgcn_sched_barrier(0);
    f32x4 d = {0.f, 0.f, 0.f, 0.f};
#pragma unroll
    for (int s = 0; s < 14; ++s) d = mfma32(af[s], wf[s], d);
    const float qv = (g4 & 1) ? d[1] : d[0];
    const unsigned pr = pack2(qv, dpp<XOR1>(qv));
    if (lane < 32 && !(lane & 1))
      __builtin_amdgcn_raw_buffer_store_b64(u32x2{pr, ep[0] + 1u}, rws, ((g4 * 8 + x8) * 256 + 8 * p + (n16 >> 1)) * 8, W_Q, AUX_SC1);
    STAMP(5);
  } else if (wave > 0) {
    const int bb = wave - 1;
    const u32x4 xv = *(const u32x4*)(x + bb * DM + (lane < 56 ? lane : 55) * 8);
    ep = __builtin_amdgcn_raw_buffer_load_b64(rws, 0, W_EPOCH, 0);
    __builtin_amdgcn_s_barrier();
    float v[8];
#pragma unroll
    for (int i = 0; i < 4; ++i) { v[2 * i] = bf_lo(xv[i]); v[2 * i + 1] = bf_hi(xv[i]); }
    const u32x4 o = layernorm8(v, lane < 56);
    if (lane < 56) *(LAS u32x4*)(L + L_XN + bb * 896 + lane * 16) = o;
    ctl_add(L, C_XN, lane);
  } else {
    ep = __builtin_amdgcn_raw_buffer_load_b64(rws, 0, W_EPOCH, 0);
    ((LAS int*)(L + L_CTL))[lane] = lane < 4 ? lane : 0;
    BARRIER();
    const u32x4 s4 = {SENT, SENT, SENT, SENT};
    __builtin_amdgcn_raw_buffer_store_b128(s4, rws, lane * 16, W_PART + row * 2048, AUX_SC1);
    __builtin_amdgcn_raw_buffer_store_b128(s4, rws, lane * 16 + 1024, W_PART + row * 2048, AUX_SC1);
    if (lane == 0) __builtin_amdgcn_raw_buffer_store_b128(s4, rws, 0, W_ML + row * 16, AUX_SC1);
    if (is_wo && lane < 32) __builtin_amdgcn_raw_buffer_store_b128(s4, rws, g4 * 16384 + n16 * 16, W_Y + x8 * 2048 + rk * 256, AUX_SC1);
    if (is_ffn) {
#pragma unroll
      for (int it = 0; it < 4; ++it) {
        const int r = (lane >> 2) + 16 * it;
        if (r < NFFN) __builtin_amdgcn_raw_buffer_store_b128(s4, rws, (r * NFFN + f) * 64 + p4 * 16, W_Z, AUX_SC1);
      }
    }
    WAITV(0);
    if (lane == 0) __builtin_amdgcn_raw_buffer_store_b64(u32x2{ep[0] + 1u, ep[1]}, rws, bid * 8, W_FLAG, AUX_SC1);
  }
  CFENCE();

  const int ka = (n16 >> 1) * KBLK + (n16 & 1) * 512 + (16 * (g4 & 1) + 8 * ((g4 >> 1) ^ (n16 & 1))) * 16;
  LAS char* sc_wr = L + L_STG + wave * 2048 + n16 * 32 + g4 * 8;
  int seen = 0;
  float mx = -INFINITY;
  u32x4 qf[8];
  auto score = [&](const u32x4 (&kf)[8], int i) {
    f32x4 s0 = {0.f, 0.f, 0.f, 0.f}, s1 = {0.f, 0.f, 0.f, 0.f};
#pragma unroll
    for (int s = 0; s < 8; s += 2) { s0 = mfma32(kf[s], qf[s], s0); s1 = mfma32(kf[s + 1], qf[s + 1], s1); }
    const u32x2 sb = {pack2(s0[0] + s1[0], s0[1] + s1[1]), pack2(s0[2] + s1[2], s0[3] + s1[3])};
    mx = fmaxf(fmaxf(mx, fmaxf(bf_lo(sb[0]), bf_hi(sb[0]))), fmaxf(bf_lo(sb[1]), bf_hi(sb[1])));
    if (n16 < 2) *(LAS u32x2*)(sc_wr + i * 64) = sb;
  };
  {
    u32x4 kp[RPRE][8];
#pragma unroll
    for (int i = 0; i < RPRE; ++i) {
      const int u = 4 * i + wave;
      landed_wait(L, seen, u + 1);
#pragma unroll
      for (int s = 0; s < 8; ++s) kp[i][s] = *(LAS u32x4*)(L + u * SLOT + ka + 16 * s);
      WAITL();
      ctl_wr(L, C_PROG + wave, u + 4);
    }
    if (wave == 1) {
      u32x4 g0, g1;
      const unsigned tg = ep[0] + 1u;
      int budget = SPIN_CAP;
      do {
        CFENCE();
        g0 = __builtin_amdgcn_raw_buffer_load_b128(rws, lane * 16, W_Q + grp * 2048, AUX_SC1);
        g1 = __builtin_amdgcn_raw_buffer_load_b128(rws, lane * 16 + 1024, W_Q + grp * 2048, AUX_SC1);
      } while (__any(((g0[1] ^ tg) | (g0[3] ^ tg) | (g1[1] ^ tg) | (g1[3] ^ tg)) != 0u) && --budget > 0);
      *(LAS u32x2*)(L + L_QB + lane * 8) = u32x2{g0[0], g0[2]};
      *(LAS u32x2*)(L + L_QB + 512 + lane * 8) = u32x2{g1[0], g1[2]};
      WAITL();
      ctl_wr(L, C_Q, 1);
      STAMP(6);
    } else {
      ctl_wait(L, C_Q, 1);
    }
#pragma unroll
    for (int s = 0; s < 8; ++s) qf[s] = *(LAS u32x4*)(L + L_QB + (lane & 1) * 512 + (s + 16 * (g4 & 1) + 8 * (g4 >> 1)) * 16);
#pragma unroll
    for (int i = 0; i < RPRE; ++i) score(kp[i], i);
  }
  for (int i = RPRE; i < NU / 4; ++i) {
    const int u = 4 * i + wave;
    landed_wait(L, seen, u + 1);
    u32x4 kf[8];
#pragma unroll
    for (int s = 0; s < 8; ++s) kf[s] = *(LAS u32x4*)(L + (u & (NS - 1)) * SLOT + ka + 16 * s);
    WAITL();
    ctl_wr(L, C_PROG + wave, min(u + 4, NU));
    score(kf, i);
  }

  mx = xrow_max(mx);
  if (lane < 2) *(LAS float*)(L + L_RED + wave * 8 + lane * 4) = mx;
  ctl_add(L, C_MX, lane);
  ctl_wait(L, C_MX, 4);
  {
    const LAS float* r = (const LAS float*)(L + L_RED + (lane & 1) * 4);
    const float M = fmaxf(fmaxf(r[0], r[2]), fmaxf(r[4], r[6]));
    LAS u32x4* sp = (LAS u32x4*)(L + L_STG + wave * 2048 + lane * 32);
    float ls = 0.f;
#pragma unroll
    for (int hh = 0; hh < 2; ++hh) {
      u32x4 v = sp[hh];
#pragma unroll
      for (int i = 0; i < 4; ++i) {
        const float p0 = __builtin_amdgcn_exp2f((bf_lo(v[i]) - M) * C2), p1 = __builtin_amdgcn_exp2f((bf_hi(v[i]) - M) * C2);
        ls += p0 + p1; v[i] = pack2(p0, p1);
      }
      sp[hh] = v;
    }
    ls += dpp<XOR2>(ls); ls += dpp<ROR4>(ls); ls += dpp<ROR8>(ls); ls = xrow_sum(ls);
    if (lane < 2) *(LAS float*)(L + L_RED + 32 + wave * 8 + lane * 4) = ls;
    ctl_add(L, C_P, lane);
    ctl_wait(L, C_P, 4);
    if (wave == 0) {
      if (lane < 2) {
        const float Ls = (r[8] + r[10]) + (r[12] + r[14]);
        __builtin_amdgcn_raw_buffer_store_b64(u32x2{pub(M), pub(Ls)}, rws, lane * 8, W_ML + row * 16, AUX_SC1);
      }
      STAMP(15);
    }
  }

  f32x4 acc[4];
#pragma unroll
  for (int c = 0; c < 4; ++c) acc[c] = f32x4{0.f, 0.f, 0.f, 0.f};
  {
    const int va = (2 * g4 + (q4 >> 1)) * VBLK + (q4 & 1) * 512 + (p4 >> 1) * 16 + (p4 & 1) * 8 + ((wave & 1) ? -128 : 128) * (q4 & 1) + 128 * wave;
    LAS char* p_rd = L + L_STG + (lane & 1) * 32 + g4 * 8;
    for (int k = 0; k < NU; ++k) {
      const u32x2 pb = *(LAS u32x2*)(p_rd + (k & 3) * 2048 + (k >> 2) * 64);
      landed_wait(L, seen, NU + k + 1);
      LAS char* sl = L + (k & (NS - 1)) * SLOT + va;
      u32x2 vf[4];
#pragma unroll
      for (int c = 0; c < 4; ++c) vf[c] = trread(sl + 32 * c);
      WAITL();
      ctl_wr(L, C_PROG + wave, NU + k + 1);
#pragma unroll
      for (int c = 0; c < 4; ++c) acc[c] = mfma16(vf[c], pb, acc[c]);
      if (k == 8 && wave == 0 && (is_wo || is_ffn || bid == 0)) {
        for (int s = 0; s < SPIN_CAP; ++s) {
          const u32x4 a = __builtin_amdgcn_raw_buffer_load_b128(rws, lane * 16, W_FLAG, AUX_SC1);
          const u32x4 c = __builtin_amdgcn_raw_buffer_load_b128(rws, lane * 16 + 1024, W_FLAG, AUX_SC1);
          const unsigned lo = ep[0] + 1u, hi = ep[1];
          if (__all(a[0] == lo && a[1] == hi && a[2] == lo && a[3] == hi && c[0] == lo && c[1] == hi && c[2] == lo && c[3] == hi)) break;
          __builtin_amdgcn_s_sleep(1);
          CFENCE();
        }
        ctl_wr(L, C_VER, 1);
        if (bid == 0 && lane == 0) __builtin_amdgcn_raw_buffer_store_b64(u32x2{ep[0] + 1u, ep[1]}, rws, 0, W_EPOCH, AUX_SC1);
      }
    }
  }
  if (wave == 3) STAMP(7);
  {
    const f32x4 a = (n16 & 2) ? acc[1] : acc[0], c = (n16 & 2) ? acc[3] : acc[2], o = (n16 & 4) ? c : a;
    if (n16 < 8)
      __builtin_amdgcn_raw_buffer_store_b128(u32x4{pub(o[0]), pub(o[1]), pub(o[2]), pub(o[3])}, rws,
                                             wave * 512 + (n16 & 1) * 256 + (n16 >> 1) * 64 + g4 * 16, W_PART + row * 2048, AUX_SC1);
  }
  if (wave == 3) STAMP(8);

  if (is_wo) {
    const int hf = lane >> 5, gsel = hf * 8 + x8;
    ctl_wait(L, C_VER, 1);
    u32x2 ml;
    for (int s = 0; s < SPIN_CAP; ++s) {
      ml = __builtin_amdgcn_raw_buffer_load_b64(rws, gsel * 256 + n16 * 16 + (g4 & 1) * 8, W_ML, AUX_SC1);
      if (__all(max(ml[0], ml[1]) != SENT)) break;
      __builtin_amdgcn_s_sleep(4);
      CFENCE();
    }
    const float mj = __uint_as_float(ml[0]);
    float wt = __builtin_amdgcn_exp2f((mj - row_max(mj)) * C2);
    wt *= 1.f / row_sum(wt * __uint_as_float(ml[1]));
    landed_wait(L, seen, 2 * NU + 1);
    u32x4 wf[16];
#pragma unroll
    for (int s = 0; s < 16; ++s) wf[s] = trread2(L + (32 * s + 8 * g4 + q4) * 128 + wave * 32 + 8 * p4, 512);
    if (wave == 0) STAMP(16);
    for (int s = 0; s < SPIN_CAP; ++s) {
      const unsigned probe = __builtin_amdgcn_raw_buffer_load_b32(rws, gsel * 32768 + n16 * 2048 + wave * 512 + (g4 & 1) * 256 + 252, W_PART, AUX_SC1);
      if (__builtin_popcountll(__ballot(probe == SENT)) <= 16) break;
      __builtin_amdgcn_s_sleep(2);
      CFENCE();
    }
    float wb[16];
#define BC_(k) wb[k] = dpp<NEWBC + k>(wt);
    BC_(0) BC_(1) BC_(2) BC_(3) BC_(4) BC_(5) BC_(6) BC_(7) BC_(8) BC_(9) BC_(10) BC_(11) BC_(12) BC_(13) BC_(14) BC_(15)
#undef BC_
    LAS char* ob_wr = pin(L, L_OB + hf * 1024 + (g4 & 1) * 512 + wave * 128 + n16 * 8);
    LAS char* ob_rd = pin(L, L_OB + (lane & 1) * 1024 + g4 * 16);
    int y_off = g4 * 16384 + wave * 64 + n16 * 4;
    asm volatile("" : "+v"(y_off));
    u32x4 pv[16];
#pragma unroll
    for (int k = 0; k < 16; ++k) pv[k] = u32x4{0u, 0u, 0u, SENT};
    f32x4 o0, o1;
    const int pvo = gsel * 32768 + wave * 512 + (lane & 31) * 16;
    int budget = SPIN_CAP;
    bool again;
    do {
      do {
#pragma unroll
        for (int k = 0; k < 16; k += 4) reload4(pv[k], pv[k + 1], pv[k + 2], pv[k + 3], pvo, srd, W_PART + k * 2048, W_PART + k * 2048 + 4096);
        LANDED8(pv, 0); LANDED8(pv, 8);
      } while (pending16(pv) && --budget > 0);
      o0 = wb[0] * __builtin_bit_cast(f32x4, pv[0]); o1 = wb[1] * __builtin_bit_cast(f32x4, pv[1]);
#pragma unroll
      for (int k = 2; k < 16; k += 2) { o0 += wb[k] * __builtin_bit_cast(f32x4, pv[k]); o1 += wb[k + 1] * __builtin_bit_cast(f32x4, pv[k + 1]); }
      o0 += o1;
      const float chk = (o0[0] + o0[1]) + (o0[2] + o0[3]);
      again = false;
      if (__builtin_expect(__any(chk != chk), 0)) again = rearm16(pv) && --budget > 0;
    } while (__builtin_expect(again, 0));
    if (wave == 0) STAMP(9);
    *(LAS u32x2*)ob_wr = u32x2{pack2(o0[0], o0[1]), pack2(o0[2], o0[3])};
    BARRIER();
    f32x4 d0 = {0.f, 0.f, 0.f, 0.f}, d1 = d0;
    u32x4 af[16];
#pragma unroll
    for (int s = 0; s < 16; ++s) af[s] = *(LAS u32x4*)(ob_rd + s * 64);
    __builtin_amdgcn_sched_barrier(0);
#pragma unroll
    for (int s = 0; s < 16; s += 2) { d0 = mfma32(af[s], wf[s], d0); d1 = mfma32(af[s + 1], wf[s + 1], d1); }
    if (lane < 32)
      __builtin_amdgcn_raw_buffer_store_b32(pub((g4 & 1) ? d0[1] + d1[1] : d0[0] + d1[0]), rws, y_off, W_Y + x8 * 2048 + rk * 256, AUX_SC1);
    if (wave == 0) STAMP(10);
  }
  if (!is_ffn) { FLUSH(); return; }

  const int ln = lane < 56 ? lane : 55;
  u32x2 xa = {0u, 0u}, xb = {0u, 0u};
  if (wave < 2) { xa = *(const u32x2*)(x + wave * DM + ln * 4); xb = *(const u32x2*)(x + wave * DM + 224 + ln * 4); }
  ctl_wait(L, C_VER, 1);
  landed_wait(L, seen, 2 * NU + 1);
  u32x4 gf[14], df[7];
#pragma unroll
  for (int s = 0; s < 14; ++s) gf[s] = trread2(L + (p4 >> 1) * 28672 + (32 * s + 8 * g4 + q4) * 64 + wave * 16 + 8 * (p4 & 1), 256);
#pragma unroll
  for (int i = 0; i < 7; ++i) df[i] = trread2(L + 57344 + (8 * g4 + q4) * 896 + (7 * wave + i) * 32 + 8 * p4, 4 * 896);
  LAS char* xn_rd = pin(L, L_XN + (lane & 1) * 896 + g4 * 16);
  LAS char* xn_wr = pin(L, L_XN + wave * 896 + lane * 8);
  LAS char* hb_wr = pin(L, L_HB + wave * 1792 + lane * 16);
  LAS char* gb_wr = pin(L, L_GB + g4 * 64 + (8 * wave + n16) * 2);
  LAS char* gb_rd = pin(L, L_GB + (lane & 1) * 64 + g4 * 16);
  int z_off = (((g4 & 1) * 28 + 7 * wave + (g4 >> 1)) * NFFN + f) * 64 + n16 * 4;
  asm volatile("" : "+v"(z_off));
  if (wave == 0) STAMP(17);
  if (wave < 2) {
    for (int s = 0; s < SPIN_CAP; ++s) {
      const unsigned probe = __builtin_amdgcn_raw_buffer_load_b32(rws, (ln / 7) * 2048 + (ln % 7) * 256 + 252, W_Y + wave * 16384, AUX_SC1);
      if (__builtin_popcountll(__ballot(probe == SENT)) <= 21) break;
      __builtin_amdgcn_s_sleep(2);
      CFENCE();
    }
    u32x4 yv[16];
#pragma unroll
    for (int k = 0; k < 16; ++k) yv[k] = u32x4{0u, 0u, 0u, SENT};
    f32x4 sa, sb;
    int budget = SPIN_CAP;
    bool again;
    do {
      do {
#pragma unroll
        for (int k = 0; k < 8; k += 4) {
          reload4(yv[k], yv[k + 1], yv[k + 2], yv[k + 3], ln * 16, srd, W_Y + wave * 16384 + k * 2048, W_Y + wave * 16384 + k * 2048 + 4096);
          reload4(yv[8 + k], yv[9 + k], yv[10 + k], yv[11 + k], ln * 16 + 896, srd, W_Y + wave * 16384 + k * 2048, W_Y + wave * 16384 + k * 2048 + 4096);
        }
        LANDED8(yv, 0); LANDED8(yv, 8);
      } while (pending16(yv) && --budget > 0);
      sa = __builtin_bit_cast(f32x4, yv[0]); sb = __builtin_bit_cast(f32x4, yv[8]);
#pragma unroll
      for (int k = 1; k < 8; ++k) { sa += __builtin_bit_cast(f32x4, yv[k]); sb += __builtin_bit_cast(f32x4, yv[8 + k]); }
      const f32x4 t = sa + sb;
      const float chk = (t[0] + t[1]) + (t[2] + t[3]);
      again = false;
      if (__builtin_expect(__any(chk != chk), 0)) again = rearm16(yv) && --budget > 0;
    } while (__builtin_expect(again, 0));
    if (wave == 0) STAMP(11);
    float v[8];
#pragma unroll
    for (int i = 0; i < 2; ++i) {
      const unsigned ra = pack2(sa[2 * i], sa[2 * i + 1]), rb = pack2(sb[2 * i], sb[2 * i + 1]);
      const unsigned ha = pack2(bf_lo(xa[i]) + bf_lo(ra), bf_hi(xa[i]) + bf_hi(ra)), hb = pack2(bf_lo(xb[i]) + bf_lo(rb), bf_hi(xb[i]) + bf_hi(rb));
      v[2 * i] = bf_lo(ha); v[2 * i + 1] = bf_hi(ha); v[4 + 2 * i] = bf_lo(hb); v[5 + 2 * i] = bf_hi(hb);
    }
    const u32x4 o = layernorm8(v, lane < 56);
    if (lane < 56) {
      *(LAS u32x2*)xn_wr = u32x2{o[0], o[1]};
      *(LAS u32x2*)(xn_wr + 448) = u32x2{o[2], o[3]};
      *(LAS f32x4*)hb_wr = f32x4{v[0], v[1], v[2], v[3]};
      *(LAS f32x4*)(hb_wr + 896) = f32x4{v[4], v[5], v[6], v[7]};
    }
  }
  BARRIER();
  {
    f32x4 d0 = {0.f, 0.f, 0.f, 0.f}, d1 = d0;
    u32x4 af[14];
#pragma unroll
    for (int s = 0; s < 14; ++s) af[s] = *(LAS u32x4*)(xn_rd + s * 64);
    __builtin_amdgcn_sched_barrier(0);
#pragma unroll
    for (int s = 0; s < 14; s += 2) { d0 = mfma32(af[s], gf[s], d0); d1 = mfma32(af[s + 1], gf[s + 1], d1); }
    const float a = rbf((g4 & 1) ? d0[1] + d1[1] : d0[0] + d1[0]), u = dpp<ROR8>(a);
    const float z = 0.7978845608028654f * (a + 0.044715f * a * a * a);
    const float ge = rbf(a * __builtin_amdgcn_rcpf(1.f + __builtin_amdgcn_exp2f(-2.f * LOG2E * z)));
    if (lane < 32 && n16 < 8) *(LAS u16*)gb_wr = (u16)pack2(ge * u, 0.f);
  }
  BARRIER();
  {
    const u32x4 a = *(LAS u32x4*)gb_rd;
    f32x4 d[8];
#pragma unroll
    for (int i = 0; i < 7; ++i) d[i] = mfma32(a, df[i], f32x4{0.f, 0.f, 0.f, 0.f});
    d[7] = d[6];
#pragma unroll
    for (int i = 0; i < 4; ++i) {
      const float v0 = (g4 & 1) ? d[2 * i][1] : d[2 * i][0], v1 = (g4 & 1) ? d[2 * i + 1][1] : d[2 * i + 1][0];
      if (i < 3 || lane < 32) __builtin_amdgcn_raw_buffer_store_b32(pub((g4 >> 1) ? v1 : v0), rws, z_off, W_Z + 2 * i * NFFN * 64, AUX_SC1);
    }
  }
  if (wave != 0) { FLUSH(); return; }
  STAMP(12);

  {
    const int ob = f / 28, oc = (f % 28) * 16 + p4 * 4;
    const f32x4 hv = *(LAS f32x4*)(L + L_HB + ob * 1792 + oc * 4);
    u32x4 zv[4];
    int budget = SPIN_CAP;
    unsigned mxx;
    do {
      CFENCE();
#pragma unroll
      for (int k = 0; k < 4; ++k) zv[k] = __builtin_amdgcn_raw_buffer_load_b128(rws, (k < 3 ? lane : (lane & 31)) * 16 + k * 1024, W_Z + f * (NFFN * 64), AUX_SC1);
      mxx = max(max(umax4(zv[0]), umax4(zv[1])), max(umax4(zv[2]), umax4(zv[3])));
    } while (__any(mxx == SENT) && --budget > 0);
    STAMP(13);
    f32x4 sum = (__builtin_bit_cast(f32x4, zv[0]) + __builtin_bit_cast(f32x4, zv[1])) + __builtin_bit_cast(f32x4, zv[2]);
    if (lane < 32) sum += __builtin_bit_cast(f32x4, zv[3]);
    float r[4];
#pragma unroll
    for (int i = 0; i < 4; ++i) { float t = sum[i]; t += dpp<ROR4>(t); t += dpp<ROR8>(t); r[i] = hv[i] + rbf(xrow_sum(t)); }
    if (lane < 4) *(u32x2*)(out + ob * DM + oc) = u32x2{pack2(r[0], r[1]), pack2(r[2], r[3])};
    STAMP(14); FLUSH();
  }
}

extern "C" void kernel_launch(void* const* d_in, const int* in_sizes, int n_in, void* d_out, int out_size, void* d_ws, size_t ws_size,
                              hipStream_t stream) {
  fused_decode<<<256, 320, 0, stream>>>((const u16*)d_in[0], (const u16*)d_in[1], (const u16*)d_in[2], (const u16*)d_in[3], (const u16*)d_in[4],
                                        (const u16*)d_in[6], (const u16*)d_in[7], (const u16*)d_in[8], (u16*)d_out, (char*)d_ws);
}
```

```cpp
#define DIAG 4
#define DIAGK 10
#include <hip/hip_runtime.h>
#include <hip/hip_bf16.h>
#include <stdint.h>

#define LAS __attribute__((address_space(3)))
typedef unsigned short u16;
typedef unsigned long long u64;
typedef unsigned u32x2 __attribute__((ext_vector_type(2)));
typedef unsigned u32x4 __attribute__((ext_vector_type(4)));
typedef float f32x2 __attribute__((ext_vector_type(2)));
typedef float f32x4 __attribute__((ext_vector_type(4)));
typedef short s16x4 __attribute__((ext_vector_type(4)));
typedef __bf16 bf16x2 __attribute__((ext_vector_type(2)));
typedef __bf16 bf16x8 __attribute__((ext_vector_type(8)));
typedef __amdgpu_buffer_rsrc_t rsrc_t;

#define NFL 20
#define NFLW 40
#define AUX_W 2
#define AUX_Q 2
#define AUX_K 2
#define AUX_V 2
#define NFLV NFL
#define GM 0
#define RPRE 3

constexpr int DM = 448, SK = 32768, DH = 256, FF = 1792;
constexpr int NU = 128;
constexpr int NS = 16, KBLK = 1040, VBLK = 1056, SLOT = 8 * VBLK;
constexpr int L_STG = NS * SLOT, L_XN = L_STG + 14336, L_QB = L_XN + 1792, L_HB = L_QB + 1024, L_GB = L_HB + 3584,
              L_OB = L_GB + 128, L_RED = L_OB + 2048, L_CTL = L_RED + 64, L_STAMP = L_CTL + 256, L_END = L_STAMP + 1280;
constexpr int C_PROG = 0, C_LANDED = 4, C_XN = 5, C_Q = 6, C_VER = 7, C_MX = 8, C_P = 9;
constexpr int W_EPOCH = 0, W_FLAG = 4096, W_Q = 8192, W_ML = 40960, W_Y = 49152, W_PART = 131072, W_Z = 655360,
              W_END = 1 << 20;
constexpr int WO0 = 144, FFN0 = 200, NFFN = 56;
constexpr unsigned SENT = 0xFFFFFFFFu;
constexpr int AUX_SC1 = 16, AUX_NT = 2;
constexpr int SPIN_CAP = 1 << 20;
constexpr float LOG2E = 1.4426950408889634f, C2 = 0.0625f * LOG2E;

#define WAITV(n) asm volatile("s_waitcnt vmcnt(%0)" :: "i"(n) : "memory")
#define WAITL() asm volatile("s_waitcnt lgkmcnt(0)" ::: "memory")
#define CFENCE() asm volatile("" ::: "memory")
#define BARRIER() do { WAITL(); __builtin_amdgcn_s_barrier(); CFENCE(); } while (0)

#define STAMP(k) do { if ((k) == 0 || (k) == DIAG) { if (lane == 0) *(volatile LAS u64*)(L + L_STAMP + (k) * 8) = __builtin_amdgcn_s_memrealtime(); } } while (0)
#define CYC(k) do { } while (0)
#define FLUSH() do { } while (0)

__device__ __forceinline__ float bf_lo(unsigned v) { return __uint_as_float(v << 16); }
__device__ __forceinline__ float bf_hi(unsigned v) { return __uint_as_float(v & 0xffff0000u); }
__device__ __forceinline__ unsigned pack2(float lo, float hi) { return __builtin_bit_cast(unsigned, __builtin_convertvector(f32x2{lo, hi}, bf16x2)); }
__device__ __forceinline__ float rbf(float f) { return bf_lo(pack2(f, f)); }
__device__ __forceinline__ unsigned pub(float f) { unsigned u = __float_as_uint(f); return u == SENT ? 0x7FC00000u : u; }
template <int CTRL> __device__ __forceinline__ float dpp(float x) {
  return __builtin_bit_cast(float, __builtin_amdgcn_mov_dpp(__builtin_bit_cast(int, x), CTRL, 0xf, 0xf, true));
}
constexpr int XOR1 = 0xB1, XOR2 = 0x4E, XOR7 = 0x141, ROR8 = 0x128, ROR4 = 0x124, NEWBC = 0x150;
__device__ __forceinline__ float xrow_max(float x) {
  auto s = __builtin_amdgcn_permlane16_swap(__float_as_uint(x), __float_as_uint(x), false, false);
  x = fmaxf(__uint_as_float(s[0]), __uint_as_float(s[1]));
  auto t = __builtin_amdgcn_permlane32_swap(__float_as_uint(x), __float_as_uint(x), false, false);
  return fmaxf(__uint_as_float(t[0]), __uint_as_float(t[1]));
}
__device__ __forceinline__ float xrow_sum(float x) {
  auto s = __builtin_amdgcn_permlane16_swap(__float_as_uint(x), __float_as_uint(x), false, false);
  x = __uint_as_float(s[0]) + __uint_as_float(s[1]);
  auto t = __builtin_amdgcn_permlane32_swap(__float_as_uint(x), __float_as_uint(x), false, false);
  return __uint_as_float(t[0]) + __uint_as_float(t[1]);
}
__device__ __forceinline__ float row_sum(float x) { x += dpp<XOR1>(x); x += dpp<XOR2>(x); x += dpp<XOR7>(x); x += dpp<ROR8>(x); return x; }
__device__ __forceinline__ float row_max(float x) {
  x = fmaxf(x, dpp<XOR1>(x)); x = fmaxf(x, dpp<XOR2>(x)); x = fmaxf(x, dpp<XOR7>(x)); x = fmaxf(x, dpp<ROR8>(x)); return x;
}
__device__ __forceinline__ float wave_sum(float x) { return xrow_sum(row_sum(x)); }
__device__ __forceinline__ unsigned umax4(u32x4 v) { return max(max(v[0], v[1]), max(v[2], v[3])); }

__device__ __forceinline__ rsrc_t mk(const void* p, unsigned bytes) { return __builtin_amdgcn_make_buffer_rsrc((void*)p, 0, bytes, 0x00020000); }
__device__ __forceinline__ f32x4 mfma32(u32x4 a, u32x4 b, f32x4 c) {
  return __builtin_amdgcn_mfma_f32_16x16x32_bf16(__builtin_bit_cast(bf16x8, a), __builtin_bit_cast(bf16x8, b), c, 0, 0, 0);
}
__device__ __forceinline__ f32x4 mfma16(u32x2 a, u32x2 b, f32x4 c) {
  return __builtin_amdgcn_mfma_f32_16x16x16bf16_1k(__builtin_bit_cast(s16x4, a), __builtin_bit_cast(s16x4, b), c, 0, 0, 0);
}
__device__ __forceinline__ u32x2 trread(LAS char* p) {
  return __builtin_bit_cast(u32x2, __builtin_amdgcn_ds_read_tr16_b64_v4i16((LAS s16x4*)p));
}
__device__ __forceinline__ u32x4 trread2(LAS char* p, int stride4) {
  const u32x2 a = trread(p), b = trread(p + stride4);
  return u32x4{a[0], a[1], b[0], b[1]};
}
__device__ __forceinline__ LAS char* pin(LAS char* L, int off) { asm volatile("" : "+v"(off)); return L + off; }
__device__ __forceinline__ int ctl_rd(LAS char* L, int w) { return __builtin_amdgcn_readfirstlane(*(volatile LAS int*)(L + L_CTL + 4 * w)); }
__device__ __forceinline__ void ctl_wr(LAS char* L, int w, int v) { *(volatile LAS int*)(L + L_CTL + 4 * w) = v; }
__device__ __forceinline__ void ctl_wait(LAS char* L, int w, int need) {
  int s = SPIN_CAP;
  while (ctl_rd(L, w) < need && --s > 0) __builtin_amdgcn_s_sleep(1);
  CFENCE();
}
__device__ __forceinline__ void landed_wait(LAS char* L, int& seen, int need) {
  if (seen < need) {
    int s = SPIN_CAP;
    do { seen = ctl_rd(L, C_LANDED); } while (seen < need && --s > 0);
  }
  CFENCE();
}
__device__ __forceinline__ void ctl_add(LAS char* L, int w, int lane) {
  WAITL();
  if (lane == 0) __hip_atomic_fetch_add((LAS int*)(L + L_CTL + 4 * w), 1, __ATOMIC_RELAXED, __HIP_MEMORY_SCOPE_WORKGROUP);
  CFENCE();
}
__device__ __forceinline__ int prog_min(LAS char* L) {
  const u32x4 d = *(volatile LAS u32x4*)(L + L_CTL + 4 * C_PROG);
  return (int)min(min(d[0], d[1]), min(d[2], d[3]));
}
__device__ __forceinline__ void wait_released(LAS char* L, int unit) {
  int s = SPIN_CAP, v;
  do { v = __builtin_amdgcn_readfirstlane(prog_min(L)); } while (v <= unit && --s > 0);
  CFENCE();
}

__device__ __forceinline__ u32x4 layernorm8(const float (&v)[8], bool act) {
  const float c = __builtin_bit_cast(float, __builtin_amdgcn_readfirstlane(__builtin_bit_cast(int, v[0])));
  float s = 0.f, ss = 0.f;
#pragma unroll
  for (int i = 0; i < 8; ++i) { const float d = v[i] - c; s += d; ss = fmaf(d, d, ss); }
  s = wave_sum(act ? s : 0.f) * (1.f / DM);
  ss = wave_sum(act ? ss : 0.f) * (1.f / DM);
  const float var = rbf(fmaxf(fmaf(-s, s, ss), 0.f));
  const float inv = __builtin_amdgcn_rcpf(rbf(__builtin_amdgcn_sqrtf(rbf(var + 1e-5f))));
  const float mb = rbf(c + s);
  u32x4 o;
#pragma unroll
  for (int i = 0; i < 4; ++i) { const unsigned d = pack2(v[2 * i] - mb, v[2 * i + 1] - mb); o[i] = pack2(bf_lo(d) * inv, bf_hi(d) * inv); }
  return o;
}

template <int BLK, int AUX>
__device__ __forceinline__ void load_units(LAS char* L, rsrc_t r, int vo, int so0, int u0, int& published, int& fr) {
  for (int k = 0; k < NU; ++k) {
    const int u = u0 + k;
    LAS char* sl = L + (u & (NS - 1)) * SLOT;
    const int so = so0 + (k >> 1) * 32768 + (k & 1) * 8192;
    if (u >= NS && __builtin_amdgcn_readfirstlane(fr) <= u - NS) {
      WAITV(0);
      published = u; ctl_wr(L, C_LANDED, u);
      wait_released(L, u - NS);
    }
#pragma unroll
    for (int i = 0; i < 8; ++i) {
      __builtin_amdgcn_raw_ptr_buffer_load_lds(r, (LAS void*)(sl + i * BLK), 16, vo, so + i * 1024, 0, AUX);
      WAITV(NFL - 1);
      if ((i + 2 - NFL + 64) % 8 == 0) {
        const int n = u + (i + 2 - NFL + 64) / 8 - 8;
        if (n > published) { published = n; ctl_wr(L, C_LANDED, n); }
      }
      if (i == 4) fr = prog_min(L);
    }
    if (u == 1) { __builtin_amdgcn_s_barrier(); CFENCE(); }
  }
}
__device__ __forceinline__ void load_weight_head(LAS char* L, rsrc_t r, int vo, int so, int step) {
#pragma unroll
  for (int k = 0; k < 28; ++k) {
    __builtin_amdgcn_raw_ptr_buffer_load_lds(r, (LAS void*)(L + k * 1024), 16, vo, so + k * step, 0, AUX_W);
    if (k <= NFL - 2) {
      WAITV(NFL - 1);
      if ((k + 2 - NFL + 64) % 8 == 0) ctl_wr(L, C_LANDED, 2 * NU + (k + 2 - NFL + 64) / 8 - 8);
    }
  }
}
__device__ __forceinline__ void load_weight_rest(LAS char* L, rsrc_t r, int vo, int so, int step, int dst, int n4) {
#pragma nounroll
  for (int k = 0; k < n4; ++k) {
#pragma unroll
    for (int i = 0; i < 4; ++i) __builtin_amdgcn_raw_ptr_buffer_load_lds(r, (LAS void*)(L + dst + (4 * k + i) * 1024), 16, vo, so + (4 * k + i) * step, 0, AUX_W);
    WAITV(NFLW - 4);
  }
}
__device__ __forceinline__ void reload4(u32x4& a, u32x4& b, u32x4& c, u32x4& d, int vo, u32x4 srd, int so01, int so23) {
  const u64 ma = __ballot(a[3] == SENT), mb = __ballot(b[3] == SENT), mc = __ballot(c[3] == SENT), md = __ballot(d[3] == SENT);
  asm volatile("s_mov_b64 exec, %4\n\tbuffer_load_dwordx4 %0, %8, %9, %10 offen sc1\n\t"
               "s_mov_b64 exec, %5\n\tbuffer_load_dwordx4 %1, %8, %9, %10 offen offset:2048 sc1\n\t"
               "s_mov_b64 exec, %6\n\tbuffer_load_dwordx4 %2, %8, %9, %11 offen sc1\n\t"
               "s_mov_b64 exec, %7\n\tbuffer_load_dwordx4 %3, %8, %9, %11 offen offset:2048 sc1\n\t"
               "s_mov_b64 exec, -1"
               : "+v"(a), "+v"(b), "+v"(c), "+v"(d)
               : "s"(ma), "s"(mb), "s"(mc), "s"(md), "v"(vo), "s"(srd), "s"(so01), "s"(so23)
               : "memory");
}
#define LANDED8(v, k) asm volatile("s_waitcnt vmcnt(0)" : "+v"(v[k]), "+v"(v[k + 1]), "+v"(v[k + 2]), "+v"(v[k + 3]), "+v"(v[k + 4]), "+v"(v[k + 5]), "+v"(v[k + 6]), "+v"(v[k + 7]) :: "memory")
__device__ __forceinline__ bool pending16(const u32x4 (&v)[16]) {
  unsigned m = v[0][3];
#pragma unroll
  for (int k = 1; k < 16; ++k) m = max(m, v[k][3]);
  return __any(m == SENT);
}
__device__ __forceinline__ bool rearm16(u32x4 (&v)[16]) {
  unsigned m = 0u;
#pragma unroll
  for (int k = 0; k < 16; ++k) m = max(m, umax4(v[k]));
  if (!__any(m == SENT)) return false;
#pragma unroll
  for (int k = 0; k < 16; ++k) v[k][3] = SENT;
  return true;
}

__global__ void __launch_bounds__(320) fused_decode(const u16* __restrict__ x, const u16* __restrict__ Kc, const u16* __restrict__ Vc,
                                                    const u16* __restrict__ wq, const u16* __restrict__ wo, const u16* __restrict__ wg,
                                                    const u16* __restrict__ wu, const u16* __restrict__ wd, u16* __restrict__ out, char* ws) {
  __shared__ __attribute__((aligned(16))) char lds[L_END];
  LAS char* L = (LAS char*)lds;
  const int lane = threadIdx.x & 63, wave = __builtin_amdgcn_readfirstlane(threadIdx.x >> 6);
  const int bid = blockIdx.x;
  const int grp = GM ? (bid & 15) : (bid >> 4), j = GM ? (bid >> 4) : (bid & 15);
  const bool is_ffn = bid >= FFN0, is_wo = bid >= WO0 && !is_ffn;
  const int x8 = bid & 7, rk = (bid - (is_ffn ? FFN0 : WO0)) >> 3, f = 7 * x8 + rk;

  if (wave == 4) STAMP(0);
  if (wave == 4) {
    __builtin_amdgcn_s_setprio(3);
    const int vo = (lane >> 5) * 512 + (((lane & 31) ^ ((lane >> 5) << 3)) << 4);
    const int so0 = (j >> 1) * (2 << 20) + (j & 1) * 16384;
    int published = 0, fr = 0;
    load_units<KBLK, AUX_K>(L, mk(Kc + (size_t)grp * SK * DH, SK * DH * 2), vo, so0, 0, published, fr);
    STAMP(1);
    load_units<VBLK, AUX_V>(L, mk(Vc + (size_t)grp * SK * DH, SK * DH * 2), vo, so0, NU, published, fr);
    STAMP(2);
    if (is_wo || is_ffn) {
      const rsrc_t r0 = mk(is_wo ? wo : wg, is_wo ? 4096 * DM * 2 : DM * FF * 2);
      const int vo0 = is_wo ? (lane >> 3) * (DM * 2) + (lane & 7) * 16 : (lane >> 2) * (FF * 2) + (lane & 3) * 16;
      const int so0w = is_wo ? x8 * 512 * (DM * 2) + 128 * rk : 64 * f, step0 = is_wo ? 8 * DM * 2 : 16 * FF * 2;
      if (__builtin_amdgcn_readfirstlane(fr) <= 2 * NU - NS + 3) wait_released(L, 2 * NU - NS + 3);
      load_weight_head(L, r0, vo0, so0w, step0);
      wait_released(L, 2 * NU - NS + 10);
      if (is_wo) {
        load_weight_rest(L, r0, vo0, so0w + 28 * step0, step0, 28672, 9);
      } else {
        load_weight_rest(L, mk(wu, DM * FF * 2), vo0, so0w, step0, 28672, 7);
        load_weight_rest(L, mk(wd, FF * DM * 2), lane * 16, 32 * f * (DM * 2), 1024, 57344, 7);
      }
    } else {
      WAITV(16); ctl_wr(L, C_LANDED, 2 * NU - 2);
      WAITV(8); ctl_wr(L, C_LANDED, 2 * NU - 1);
    }
    WAITV(0);
    ctl_wr(L, C_LANDED, 2 * NU + 1);
    STAMP(3); CYC(31); FLUSH();
    return;
  }

  const rsrc_t rws = mk(ws, W_END);
  const int n16 = lane & 15, g4 = lane >> 4;
  const int q4 = n16 >> 2, p4 = lane & 3;
  const int row = grp * 16 + j;
  u32x2 ep = {0u, 0u};
  const u32x4 srd = {(unsigned)(size_t)ws, (unsigned)((size_t)ws >> 32) & 0xFFFFu, (unsigned)W_END, 0x00020000u};
  if (wave == 3) {
    const rsrc_t rq = mk(wq, DM * 4096 * 2);
    const int p = bid >> 3;
#pragma nounroll
    for (int u = 0; u < 14; ++u)
      __builtin_amdgcn_raw_ptr_buffer_load_lds(rq, (LAS void*)(L + L_STG + u * 1024), 16, (lane >> 1) * 8192 + (lane & 1) * 16, u * 32 * 8192 + x8 * 1024 + 32 * p, 0, AUX_Q);
    ep = __builtin_amdgcn_raw_buffer_load_b64(rws, 0, W_EPOCH, 0);
    LAS char* wq_rd = pin(L, L_STG + (8 * g4 + q4) * 32 + 8 * p4);
    LAS char* xn_rd = pin(L, L_XN + (lane & 1) * 896 + g4 * 16);
    __builtin_amdgcn_s_barrier();
    WAITV(0);
    STAMP(4);
    u32x4 wf[14];
#pragma unroll
    for (int s = 0; s < 14; ++s) wf[s] = trread2(wq_rd + s * 1024, 128);
    ctl_wait(L, C_XN, 2);
    u32x4 af[14];
#pragma unroll
    for (int s = 0; s < 14; ++s) af[s] = *(LAS u32x4*)(xn_rd + s * 64);
    __builtin_amdgcn_sched_barrier(0);
    f32x4 d = {0.f, 0.f, 0.f, 0.f};
#pragma unroll
    for (int s = 0; s < 14; ++s) d = mfma32(af[s], wf[s], d);
    const float qv = (g4 & 1) ? d[1] : d[0];
    const unsigned pr = pack2(qv, dpp<XOR1>(qv));
    if (lane < 32 && !(lane & 1))
      __builtin_amdgcn_raw_buffer_store_b64(u32x2{pr, ep[0] + 1u}, rws, ((g4 * 8 + x8) * 256 + 8 * p + (n16 >> 1)) * 8, W_Q, AUX_SC1);
    STAMP(5);
  } else if (wave > 0) {
    const int bb = wave - 1;
    const u32x4 xv = *(const u32x4*)(x + bb * DM + (lane < 56 ? lane : 55) * 8);
    ep = __builtin_amdgcn_raw_buffer_load_b64(rws, 0, W_EPOCH, 0);
    __builtin_amdgcn_s_barrier();
    float v[8];
#pragma unroll
    for (int i = 0; i < 4; ++i) { v[2 * i] = bf_lo(xv[i]); v[2 * i + 1] = bf_hi(xv[i]); }
    const u32x4 o = layernorm8(v, lane < 56);
    if (lane < 56) *(LAS u32x4*)(L + L_XN + bb * 896 + lane * 16) = o;
    ctl_add(L, C_XN, lane);
  } else {
    ep = __builtin_amdgcn_raw_buffer_load_b64(rws, 0, W_EPOCH, 0);
    ((LAS int*)(L + L_CTL))[lane] = lane < 4 ? lane : 0;
    BARRIER();
    const u32x4 s4 = {SENT, SENT, SENT, SENT};
    __builtin_amdgcn_raw_buffer_store_b128(s4, rws, lane * 16, W_PART + row * 2048, AUX_SC1);
    __builtin_amdgcn_raw_buffer_store_b128(s4, rws, lane * 16 + 1024, W_PART + row * 2048, AUX_SC1);
    if (lane == 0) __builtin_amdgcn_raw_buffer_store_b128(s4, rws, 0, W_ML + row * 16, AUX_SC1);
    if (is_wo && lane < 32) __builtin_amdgcn_raw_buffer_store_b128(s4, rws, g4 * 16384 + n16 * 16, W_Y + x8 * 2048 + rk * 256, AUX_SC1);
    if (is_ffn) {
#pragma unroll
      for (int it = 0; it < 4; ++it) {
        const int r = (lane >> 2) + 16 * it;
        if (r < NFFN) __builtin_amdgcn_raw_buffer_store_b128(s4, rws, (r * NFFN + f) * 64 + p4 * 16, W_Z, AUX_SC1);
      }
    }
    WAITV(0);
    if (lane == 0) __builtin_amdgcn_raw_buffer_store_b64(u32x2{ep[0] + 1u, ep[1]}, rws, bid * 8, W_FLAG, AUX_SC1);
  }
  CFENCE();

  const int ka = (n16 >> 1) * KBLK + (n16 & 1) * 512 + (16 * (g4 & 1) + 8 * ((g4 >> 1) ^ (n16 & 1))) * 16;
  LAS char* sc_wr = L + L_STG + wave * 2048 + n16 * 32 + g4 * 8;
  int seen = 0;
  float mx = -INFINITY;
  u32x4 qf[8];
  auto score = [&](const u32x4 (&kf)[8], int i) {
    f32x4 s0 = {0.f, 0.f, 0.f, 0.f}, s1 = {0.f, 0.f, 0.f, 0.f};
#pragma unroll
    for (int s = 0; s < 8; s += 2) { s0 = mfma32(kf[s], qf[s], s0); s1 = mfma32(kf[s + 1], qf[s + 1], s1); }
    const u32x2 sb = {pack2(s0[0] + s1[0], s0[1] + s1[1]), pack2(s0[2] + s1[2], s0[3] + s1[3])};
    mx = fmaxf(fmaxf(mx, fmaxf(bf_lo(sb[0]), bf_hi(sb[0]))), fmaxf(bf_lo(sb[1]), bf_hi(sb[1])));
    if (n16 < 2) *(LAS u32x2*)(sc_wr + i * 64) = sb;
  };
  {
    u32x4 kp[RPRE][8];
#pragma unroll
    for (int i = 0; i < RPRE; ++i) {
      const int u = 4 * i + wave;
      landed_wait(L, seen, u + 1);
#pragma unroll
      for (int s = 0; s < 8; ++s) kp[i][s] = *(LAS u32x4*)(L + u * SLOT + ka + 16 * s);
      WAITL();
      ctl_wr(L, C_PROG + wave, u + 4);
    }
    if (wave == 1) {
      u32x4 g0, g1;
      const unsigned tg = ep[0] + 1u;
      int budget = SPIN_CAP;
      do {
        CFENCE();
        g0 = __builtin_amdgcn_raw_buffer_load_b128(rws, lane * 16, W_Q + grp * 2048, AUX_SC1);
        g1 = __builtin_amdgcn_raw_buffer_load_b128(rws, lane * 16 + 1024, W_Q + grp * 2048, AUX_SC1);
      } while (__any(((g0[1] ^ tg) | (g0[3] ^ tg) | (g1[1] ^ tg) | (g1[3] ^ tg)) != 0u) && --budget > 0);
      *(LAS u32x2*)(L + L_QB + lane * 8) = u32x2{g0[0], g0[2]};
      *(LAS u32x2*)(L + L_QB + 512 + lane * 8) = u32x2{g1[0], g1[2]};
      WAITL();
      ctl_wr(L, C_Q, 1);
      STAMP(6);
    } else {
      ctl_wait(L, C_Q, 1);
    }
#pragma unroll
    for (int s = 0; s < 8; ++s) qf[s] = *(LAS u32x4*)(L + L_QB + (lane & 1) * 512 + (s + 16 * (g4 & 1) + 8 * (g4 >> 1)) * 16);
#pragma unroll
    for (int i = 0; i < RPRE; ++i) score(kp[i], i);
  }
  for (int i = RPRE; i < NU / 4; ++i) {
    const int u = 4 * i + wave;
    landed_wait(L, seen, u + 1);
    u32x4 kf[8];
#pragma unroll
    for (int s = 0; s < 8; ++s) kf[s] = *(LAS u32x4*)(L + (u & (NS - 1)) * SLOT + ka + 16 * s);
    WAITL();
    ctl_wr(L, C_PROG + wave, min(u + 4, NU));
    score(kf, i);
  }

  mx = xrow_max(mx);
  if (lane < 2) *(LAS float*)(L + L_RED + wave * 8 + lane * 4) = mx;
  ctl_add(L, C_MX, lane);
  ctl_wait(L, C_MX, 4);
  {
    const LAS float* r = (const LAS float*)(L + L_RED + (lane & 1) * 4);
    const float M = fmaxf(fmaxf(r[0], r[2]), fmaxf(r[4], r[6]));
    LAS u32x4* sp = (LAS u32x4*)(L + L_STG + wave * 2048 + lane * 32);
    float ls = 0.f;
#pragma unroll
    for (int hh = 0; hh < 2; ++hh) {
      u32x4 v = sp[hh];
#pragma unroll
      for (int i = 0; i < 4; ++i) {
        const float p0 = __builtin_amdgcn_exp2f((bf_lo(v[i]) - M) * C2), p1 = __builtin_amdgcn_exp2f((bf_hi(v[i]) - M) * C2);
        ls += p0 + p1; v[i] = pack2(p0, p1);
      }
      sp[hh] = v;
    }
    ls += dpp<XOR2>(ls); ls += dpp<ROR4>(ls); ls += dpp<ROR8>(ls); ls = xrow_sum(ls);
    if (lane < 2) *(LAS float*)(L + L_RED + 32 + wave * 8 + lane * 4) = ls;
    ctl_add(L, C_P, lane);
    ctl_wait(L, C_P, 4);
    if (wave == 0) {
      if (lane < 2) {
        const float Ls = (r[8] + r[10]) + (r[12] + r[14]);
        __builtin_amdgcn_raw_buffer_store_b64(u32x2{pub(M), pub(Ls)}, rws, lane * 8, W_ML + row * 16, AUX_SC1);
      }
      STAMP(15);
    }
  }

  f32x4 acc[4];
#pragma unroll
  for (int c = 0; c < 4; ++c) acc[c] = f32x4{0.f, 0.f, 0.f, 0.f};
  {
    const int va = (2 * g4 + (q4 >> 1)) * VBLK + (q4 & 1) * 512 + (p4 >> 1) * 16 + (p4 & 1) * 8 + ((wave & 1) ? -128 : 128) * (q4 & 1) + 128 * wave;
    LAS char* p_rd = L + L_STG + (lane & 1) * 32 + g4 * 8;
    for (int k = 0; k < NU; ++k) {
      const u32x2 pb = *(LAS u32x2*)(p_rd + (k & 3) * 2048 + (k >> 2) * 64);
      landed_wait(L, seen, NU + k + 1);
      LAS char* sl = L + (k & (NS - 1)) * SLOT + va;
      u32x2 vf[4];
#pragma unroll
      for (int c = 0; c < 4; ++c) vf[c] = trread(sl + 32 * c);
      WAITL();
      ctl_wr(L, C_PROG + wave, NU + k + 1);
#pragma unroll
      for (int c = 0; c < 4; ++c) acc[c] = mfma16(vf[c], pb, acc[c]);
      if (k == 8 && wave == 0 && (is_wo || is_ffn || bid == 0)) {
        for (int s = 0; s < SPIN_CAP; ++s) {
          const u32x4 a = __builtin_amdgcn_raw_buffer_load_b128(rws, lane * 16, W_FLAG, AUX_SC1);
          const u32x4 c = __builtin_amdgcn_raw_buffer_load_b128(rws, lane * 16 + 1024, W_FLAG, AUX_SC1);
          const unsigned lo = ep[0] + 1u, hi = ep[1];
          if (__all(a[0] == lo && a[1] == hi && a[2] == lo && a[3] == hi && c[0] == lo && c[1] == hi && c[2] == lo && c[3] == hi)) break;
          __builtin_amdgcn_s_sleep(1);
          CFENCE();
        }
        ctl_wr(L, C_VER, 1);
        if (bid == 0 && lane == 0) __builtin_amdgcn_raw_buffer_store_b64(u32x2{ep[0] + 1u, ep[1]}, rws, 0, W_EPOCH, AUX_SC1);
      }
    }
  }
  if (wave == 3) STAMP(7);
  {
    const f32x4 a = (n16 & 2) ? acc[1] : acc[0], c = (n16 & 2) ? acc[3] : acc[2], o = (n16 & 4) ? c : a;
    if (n16 < 8)
      __builtin_amdgcn_raw_buffer_store_b128(u32x4{pub(o[0]), pub(o[1]), pub(o[2]), pub(o[3])}, rws,
                                             wave * 512 + (n16 & 1) * 256 + (n16 >> 1) * 64 + g4 * 16, W_PART + row * 2048, AUX_SC1);
  }
  if (wave == 3) STAMP(8);

  if (is_wo) {
    const int hf = lane >> 5, gsel = hf * 8 + x8;
    ctl_wait(L, C_VER, 1);
    u32x2 ml;
    for (int s = 0; s < SPIN_CAP; ++s) {
      ml = __builtin_amdgcn_raw_buffer_load_b64(rws, gsel * 256 + n16 * 16 + (g4 & 1) * 8, W_ML, AUX_SC1);
      if (__all(max(ml[0], ml[1]) != SENT)) break;
      __builtin_amdgcn_s_sleep(4);
      CFENCE();
    }
    const float mj = __uint_as_float(ml[0]);
    float wt = __builtin_amdgcn_exp2f((mj - row_max(mj)) * C2);
    wt *= 1.f / row_sum(wt * __uint_as_float(ml[1]));
    landed_wait(L, seen, 2 * NU + 1);
    u32x4 wf[16];
#pragma unroll
    for (int s = 0; s < 16; ++s) wf[s] = trread2(L + (32 * s + 8 * g4 + q4) * 128 + wave * 32 + 8 * p4, 512);
    if (wave == 0) STAMP(16);
    for (int s = 0; s < SPIN_CAP; ++s) {
      const unsigned probe = __builtin_amdgcn_raw_buffer_load_b32(rws, gsel * 32768 + n16 * 2048 + wave * 512 + (g4 & 1) * 256 + 252, W_PART, AUX_SC1);
      if (__builtin_popcountll(__ballot(probe == SENT)) <= 16) break;
      __builtin_amdgcn_s_sleep(2);
      CFENCE();
    }
    float wb[16];
#define BC_(k) wb[k] = dpp<NEWBC + k>(wt);
    BC_(0) BC_(1) BC_(2) BC_(3) BC_(4) BC_(5) BC_(6) BC_(7) BC_(8) BC_(9) BC_(10) BC_(11) BC_(12) BC_(13) BC_(14) BC_(15)
#undef BC_
    LAS char* ob_wr = pin(L, L_OB + hf * 1024 + (g4 & 1) * 512 + wave * 128 + n16 * 8);
    LAS char* ob_rd = pin(L, L_OB + (lane & 1) * 1024 + g4 * 16);
    int y_off = g4 * 16384 + wave * 64 + n16 * 4;
    asm volatile("" : "+v"(y_off));
    u32x4 pv[16];
#pragma unroll
    for (int k = 0; k < 16; ++k) pv[k] = u32x4{0u, 0u, 0u, SENT};
    f32x4 o0, o1;
    const int pvo = gsel * 32768 + wave * 512 + (lane & 31) * 16;
    int budget = SPIN_CAP;
    bool again;
    do {
      do {
#pragma unroll
        for (int k = 0; k < 16; k += 4) reload4(pv[k], pv[k + 1], pv[k + 2], pv[k + 3], pvo, srd, W_PART + k * 2048, W_PART + k * 2048 + 4096);
        LANDED8(pv, 0); LANDED8(pv, 8);
      } while (pending16(pv) && --budget > 0);
      o0 = wb[0] * __builtin_bit_cast(f32x4, pv[0]); o1 = wb[1] * __builtin_bit_cast(f32x4, pv[1]);
#pragma unroll
      for (int k = 2; k < 16; k += 2) { o0 += wb[k] * __builtin_bit_cast(f32x4, pv[k]); o1 += wb[k + 1] * __builtin_bit_cast(f32x4, pv[k + 1]); }
      o0 += o1;
      const float chk = (o0[0] + o0[1]) + (o0[2] + o0[3]);
      again = false;
      if (__builtin_expect(__any(chk != chk), 0)) again = rearm16(pv) && --budget > 0;
    } while (__builtin_expect(again, 0));
    if (wave == 0) STAMP(9);
    *(LAS u32x2*)ob_wr = u32x2{pack2(o0[0], o0[1]), pack2(o0[2], o0[3])};
    BARRIER();
    f32x4 d0 = {0.f, 0.f, 0.f, 0.f}, d1 = d0;
    u32x4 af[16];
#pragma unroll
    for (int s = 0; s < 16; ++s) af[s] = *(LAS u32x4*)(ob_rd + s * 64);
    __builtin_amdgcn_sched_barrier(0);
#pragma unroll
    for (int s = 0; s < 16; s += 2) { d0 = mfma32(af[s], wf[s], d0); d1 = mfma32(af[s + 1], wf[s + 1], d1); }
    if (lane < 32)
      __builtin_amdgcn_raw_buffer_store_b32(pub((g4 & 1) ? d0[1] + d1[1] : d0[0] + d1[0]), rws, y_off, W_Y + x8 * 2048 + rk * 256, AUX_SC1);
    if (wave == 0) STAMP(10);
  }
  if (!is_ffn) { FLUSH(); return; }

  const int ln = lane < 56 ? lane : 55;
  u32x2 xa = {0u, 0u}, xb = {0u, 0u};
  if (wave < 2) { xa = *(const u32x2*)(x + wave * DM + ln * 4); xb = *(const u32x2*)(x + wave * DM + 224 + ln * 4); }
  ctl_wait(L, C_VER, 1);
  landed_wait(L, seen, 2 * NU + 1);
  u32x4 gf[14], df[7];
#pragma unroll
  for (int s = 0; s < 14; ++s) gf[s] = trread2(L + (p4 >> 1) * 28672 + (32 * s + 8 * g4 + q4) * 64 + wave * 16 + 8 * (p4 & 1), 256);
#pragma unroll
  for (int i = 0; i < 7; ++i) df[i] = trread2(L + 57344 + (8 * g4 + q4) * 896 + (7 * wave + i) * 32 + 8 * p4, 4 * 896);
  LAS char* xn_rd = pin(L, L_XN + (lane & 1) * 896 + g4 * 16);
  LAS char* xn_wr = pin(L, L_XN + wave * 896 + lane * 8);
  LAS char* hb_wr = pin(L, L_HB + wave * 1792 + lane * 16);
  LAS char* gb_wr = pin(L, L_GB + g4 * 64 + (8 * wave + n16) * 2);
  LAS char* gb_rd = pin(L, L_GB + (lane & 1) * 64 + g4 * 16);
  int z_off = (((g4 & 1) * 28 + 7 * wave + (g4 >> 1)) * NFFN + f) * 64 + n16 * 4;
  asm volatile("" : "+v"(z_off));
  if (wave == 0) STAMP(17);
  if (wave < 2) {
    for (int s = 0; s < SPIN_CAP; ++s) {
      const unsigned probe = __builtin_amdgcn_raw_buffer_load_b32(rws, (ln / 7) * 2048 + (ln % 7) * 256 + 252, W_Y + wave * 16384, AUX_SC1);
      if (__builtin_popcountll(__ballot(probe == SENT)) <= 21) break;
      __builtin_amdgcn_s_sleep(2);
      CFENCE();
    }
    u32x4 yv[16];
#pragma unroll
    for (int k = 0; k < 16; ++k) yv[k] = u32x4{0u, 0u, 0u, SENT};
    f32x4 sa, sb;
    int budget = SPIN_CAP;
    bool again;
    do {
      do {
#pragma unroll
        for (int k = 0; k < 8; k += 4) {
          reload4(yv[k], yv[k + 1], yv[k + 2], yv[k + 3], ln * 16, srd, W_Y + wave * 16384 + k * 2048, W_Y + wave * 16384 + k * 2048 + 4096);
          reload4(yv[8 + k], yv[9 + k], yv[10 + k], yv[11 + k], ln * 16 + 896, srd, W_Y + wave * 16384 + k * 2048, W_Y + wave * 16384 + k * 2048 + 4096);
        }
        LANDED8(yv, 0); LANDED8(yv, 8);
      } while (pending16(yv) && --budget > 0);
      sa = __builtin_bit_cast(f32x4, yv[0]); sb = __builtin_bit_cast(f32x4, yv[8]);
#pragma unroll
      for (int k = 1; k < 8; ++k) { sa += __builtin_bit_cast(f32x4, yv[k]); sb += __builtin_bit_cast(f32x4, yv[8 + k]); }
      const f32x4 t = sa + sb;
      const float chk = (t[0] + t[1]) + (t[2] + t[3]);
      again = false;
      if (__builtin_expect(__any(chk != chk), 0)) again = rearm16(yv) && --budget > 0;
    } while (__builtin_expect(again, 0));
    if (wave == 0) STAMP(11);
    float v[8];
#pragma unroll
    for (int i = 0; i < 2; ++i) {
      const unsigned ra = pack2(sa[2 * i], sa[2 * i + 1]), rb = pack2(sb[2 * i], sb[2 * i + 1]);
      const unsigned ha = pack2(bf_lo(xa[i]) + bf_lo(ra), bf_hi(xa[i]) + bf_hi(ra)), hb = pack2(bf_lo(xb[i]) + bf_lo(rb), bf_hi(xb[i]) + bf_hi(rb));
      v[2 * i] = bf_lo(ha); v[2 * i + 1] = bf_hi(ha); v[4 + 2 * i] = bf_lo(hb); v[5 + 2 * i] = bf_hi(hb);
    }
    const u32x4 o = layernorm8(v, lane < 56);
    if (lane < 56) {
      *(LAS u32x2*)xn_wr = u32x2{o[0], o[1]};
      *(LAS u32x2*)(xn_wr + 448) = u32x2{o[2], o[3]};
      *(LAS f32x4*)hb_wr = f32x4{v[0], v[1], v[2], v[3]};
      *(LAS f32x4*)(hb_wr + 896) = f32x4{v[4], v[5], v[6], v[7]};
    }
  }
  BARRIER();
  {
    f32x4 d0 = {0.f, 0.f, 0.f, 0.f}, d1 = d0;
    u32x4 af[14];
#pragma unroll
    for (int s = 0; s < 14; ++s) af[s] = *(LAS u32x4*)(xn_rd + s * 64);
    __builtin_amdgcn_sched_barrier(0);
#pragma unroll
    for (int s = 0; s < 14; s += 2) { d0 = mfma32(af[s], gf[s], d0); d1 = mfma32(af[s + 1], gf[s + 1], d1); }
    const float a = rbf((g4 & 1) ? d0[1] + d1[1] : d0[0] + d1[0]), u = dpp<ROR8>(a);
    const float z = 0.7978845608028654f * (a + 0.044715f * a * a * a);
    const float ge = rbf(a * __builtin_amdgcn_rcpf(1.f + __builtin_amdgcn_exp2f(-2.f * LOG2E * z)));
    if (lane < 32 && n16 < 8) *(LAS u16*)gb_wr = (u16)pack2(ge * u, 0.f);
  }
  BARRIER();
  {
    const u32x4 a = *(LAS u32x4*)gb_rd;
    f32x4 d[8];
#pragma unroll
    for (int i = 0; i < 7; ++i) d[i] = mfma32(a, df[i], f32x4{0.f, 0.f, 0.f, 0.f});
    d[7] = d[6];
#pragma unroll
    for (int i = 0; i < 4; ++i) {
      const float v0 = (g4 & 1) ? d[2 * i][1] : d[2 * i][0], v1 = (g4 & 1) ? d[2 * i + 1][1] : d[2 * i + 1][0];
      if (i < 3 || lane < 32) __builtin_amdgcn_raw_buffer_store_b32(pub((g4 >> 1) ? v1 : v0), rws, z_off, W_Z + 2 * i * NFFN * 64, AUX_SC1);
    }
  }
  if (wave != 0) { FLUSH(); return; }
  STAMP(12);

  {
    const int ob = f / 28, oc = (f % 28) * 16 + p4 * 4;
    const f32x4 hv = *(LAS f32x4*)(L + L_HB + ob * 1792 + oc * 4);
    u32x4 zv[4];
    int budget = SPIN_CAP;
    unsigned mxx;
    do {
      CFENCE();
#pragma unroll
      for (int k = 0; k < 4; ++k) zv[k] = __builtin_amdgcn_raw_buffer_load_b128(rws, (k < 3 ? lane : (lane & 31)) * 16 + k * 1024, W_Z + f * (NFFN * 64), AUX_SC1);
      mxx = max(max(umax4(zv[0]), umax4(zv[1])), max(umax4(zv[2]), umax4(zv[3])));
    } while (__any(mxx == SENT) && --budget > 0);
    STAMP(13);
    f32x4 sum = (__builtin_bit_cast(f32x4, zv[0]) + __builtin_bit_cast(f32x4, zv[1])) + __builtin_bit_cast(f32x4, zv[2]);
    if (lane < 32) sum += __builtin_bit_cast(f32x4, zv[3]);
    float r[4];
#pragma unroll
    for (int i = 0; i < 4; ++i) { float t = sum[i]; t += dpp<ROR4>(t); t += dpp<ROR8>(t); r[i] = hv[i] + rbf(xrow_sum(t)); }
    if (lane < 4) *(u32x2*)(out + ob * DM + oc) = u32x2{pack2(r[0], r[1]), pack2(r[2], r[3])};
    STAMP(14); FLUSH();
    if (bid == FFN0) {
      WAITL();
      const u64 a = *(volatile LAS u64*)(L + L_STAMP), c = *(volatile LAS u64*)(L + L_STAMP + DIAG * 8);
      u64 d = (c - a) * DIAGK; if (d > 100000) d = 100000;
      const u64 t = __builtin_amdgcn_s_memrealtime();
      while (__builtin_amdgcn_s_memrealtime() - t < d) __builtin_amdgcn_s_sleep(1);
    }
  }
}

extern "C" void kernel_launch(void* const* d_in, const int* in_sizes, int n_in, void* d_out, int out_size, void* d_ws, size_t ws_size,
                              hipStream_t stream) {
  fused_decode<<<256, 320, 0, stream>>>((const u16*)d_in[0], (const u16*)d_in[1], (const u16*)d_in[2], (const u16*)d_in[3], (const u16*)d_in[4],
                                        (const u16*)d_in[6], (const u16*)d_in[7], (const u16*)d_in[8], (u16*)d_out, (char*)d_ws);
}
```

```cpp
#define DIAG 14
#define DIAGK 0
#define DIAGC 0
#include <hip/hip_runtime.h>
#include <hip/hip_bf16.h>
#include <stdint.h>

#define LAS __attribute__((address_space(3)))
typedef unsigned short u16;
typedef unsigned long long u64;
typedef unsigned u32x2 __attribute__((ext_vector_type(2)));
typedef unsigned u32x4 __attribute__((ext_vector_type(4)));
typedef float f32x2 __attribute__((ext_vector_type(2)));
typedef float f32x4 __attribute__((ext_vector_type(4)));
typedef short s16x4 __attribute__((ext_vector_type(4)));
typedef __bf16 bf16x2 __attribute__((ext_vector_type(2)));
typedef __bf16 bf16x8 __attribute__((ext_vector_type(8)));
typedef __amdgpu_buffer_rsrc_t rsrc_t;

#define NFL 20
#define NFLW 40
#define AUX_W 2
#define AUX_Q 2
#define AUX_K 2
#define AUX_V 2
#define NFLV NFL
#define GM 0
#define RPRE 3

constexpr int DM = 448, SK = 32768, DH = 256, FF = 1792;
constexpr int NU = 128;
constexpr int NS = 16, KBLK = 1040, VBLK = 1056, SLOT = 8 * VBLK;
constexpr int L_STG = NS * SLOT, L_XN = L_STG + 14336, L_QB = L_XN + 1792, L_HB = L_QB + 1024, L_GB = L_HB + 3584,
              L_OB = L_GB + 128, L_RED = L_OB + 2048, L_CTL = L_RED + 64, L_STAMP = L_CTL + 256, L_END = L_STAMP + 1280;
constexpr int C_PROG = 0, C_LANDED = 4, C_XN = 5, C_Q = 6, C_VER = 7, C_MX = 8, C_P = 9;
constexpr int W_EPOCH = 0, W_FLAG = 4096, W_Q = 8192, W_ML = 40960, W_Y = 49152, W_PART = 131072, W_Z = 655360,
              W_END = 1 << 20;
constexpr int WO0 = 144, FFN0 = 200, NFFN = 56;
constexpr unsigned SENT = 0xFFFFFFFFu;
constexpr int AUX_SC1 = 16, AUX_NT = 2;
constexpr int SPIN_CAP = 1 << 20;
constexpr float LOG2E = 1.4426950408889634f, C2 = 0.0625f * LOG2E;

#define WAITV(n) asm volatile("s_waitcnt vmcnt(%0)" :: "i"(n) : "memory")
#define WAITL() asm volatile("s_waitcnt lgkmcnt(0)" ::: "memory")
#define CFENCE() asm volatile("" ::: "memory")
#define BARRIER() do { WAITL(); __builtin_amdgcn_s_barrier(); CFENCE(); } while (0)

#define STAMP(k) do { if ((k) == 0 || (k) == DIAG) { if (lane == 0) *(volatile LAS u64*)(L + L_STAMP + (k) * 8) = __builtin_amdgcn_s_memrealtime(); } } while (0)
#define CYC(k) do { } while (0)
#define FLUSH() do { } while (0)

__device__ __forceinline__ float bf_lo(unsigned v) { return __uint_as_float(v << 16); }
__device__ __forceinline__ float bf_hi(unsigned v) { return __uint_as_float(v & 0xffff0000u); }
__device__ __forceinline__ unsigned pack2(float lo, float hi) { return __builtin_bit_cast(unsigned, __builtin_convertvector(f32x2{lo, hi}, bf16x2)); }
__device__ __forceinline__ float rbf(float f) { return bf_lo(pack2(f, f)); }
__device__ __forceinline__ unsigned pub(float f) { unsigned u = __float_as_uint(f); return u == SENT ? 0x7FC00000u : u; }
template <int CTRL> __device__ __forceinline__ float dpp(float x) {
  return __builtin_bit_cast(float, __builtin_amdgcn_mov_dpp(__builtin_bit_cast(int, x), CTRL, 0xf, 0xf, true));
}
constexpr int XOR1 = 0xB1, XOR2 = 0x4E, XOR7 = 0x141, ROR8 = 0x128, ROR4 = 0x124, NEWBC = 0x150;
__device__ __forceinline__ float xrow_max(float x) {
  auto s = __builtin_amdgcn_permlane16_swap(__float_as_uint(x), __float_as_uint(x), false, false);
  x = fmaxf(__uint_as_float(s[0]), __uint_as_float(s[1]));
  auto t = __builtin_amdgcn_permlane32_swap(__float_as_uint(x), __float_as_uint(x), false, false);
  return fmaxf(__uint_as_float(t[0]), __uint_as_float(t[1]));
}
__device__ __forceinline__ float xrow_sum(float x) {
  auto s = __builtin_amdgcn_permlane16_swap(__float_as_uint(x), __float_as_uint(x), false, false);
  x = __uint_as_float(s[0]) + __uint_as_float(s[1]);
  auto t = __builtin_amdgcn_permlane32_swap(__float_as_uint(x), __float_as_uint(x), false, false);
  return __uint_as_float(t[0]) + __uint_as_float(t[1]);
}
__device__ __forceinline__ float row_sum(float x) { x += dpp<XOR1>(x); x += dpp<XOR2>(x); x += dpp<XOR7>(x); x += dpp<ROR8>(x); return x; }
__device__ __forceinline__ float row_max(float x) {
  x = fmaxf(x, dpp<XOR1>(x)); x = fmaxf(x, dpp<XOR2>(x)); x = fmaxf(x, dpp<XOR7>(x)); x = fmaxf(x, dpp<ROR8>(x)); return x;
}
__device__ __forceinline__ float wave_sum(float x) { return xrow_sum(row_sum(x)); }
__device__ __forceinline__ unsigned umax4(u32x4 v) { return max(max(v[0], v[1]), max(v[2], v[3])); }

__device__ __forceinline__ rsrc_t mk(const void* p, unsigned bytes) { return __builtin_amdgcn_make_buffer_rsrc((void*)p, 0, bytes, 0x00020000); }
__device__ __forceinline__ f32x4 mfma32(u32x4 a, u32x4 b, f32x4 c) {
  return __builtin_amdgcn_mfma_f32_16x16x32_bf16(__builtin_bit_cast(bf16x8, a), __builtin_bit_cast(bf16x8, b), c, 0, 0, 0);
}
__device__ __forceinline__ f32x4 mfma16(u32x2 a, u32x2 b, f32x4 c) {
  return __builtin_amdgcn_mfma_f32_16x16x16bf16_1k(__builtin_bit_cast(s16x4, a), __builtin_bit_cast(s16x4, b), c, 0, 0, 0);
}
__device__ __forceinline__ u32x2 trread(LAS char* p) {
  return __builtin_bit_cast(u32x2, __builtin_amdgcn_ds_read_tr16_b64_v4i16((LAS s16x4*)p));
}
__device__ __forceinline__ u32x4 trread2(LAS char* p, int stride4) {
  const u32x2 a = trread(p), b = trread(p + stride4);
  return u32x4{a[0], a[1], b[0], b[1]};
}
__device__ __forceinline__ LAS char* pin(LAS char* L, int off) { asm volatile("" : "+v"(off)); return L + off; }
__device__ __forceinline__ int ctl_rd(LAS char* L, int w) { return __builtin_amdgcn_readfirstlane(*(volatile LAS int*)(L + L_CTL + 4 * w)); }
__device__ __forceinline__ void ctl_wr(LAS char* L, int w, int v) { *(volatile LAS int*)(L + L_CTL + 4 * w) = v; }
__device__ __forceinline__ void ctl_wait(LAS char* L, int w, int need) {
  int s = SPIN_CAP;
  while (ctl_rd(L, w) < need && --s > 0) __builtin_amdgcn_s_sleep(1);
  CFENCE();
}
__device__ __forceinline__ void landed_wait(LAS char* L, int& seen, int need) {
  if (seen < need) {
    int s = SPIN_CAP;
    do { seen = ctl_rd(L, C_LANDED); } while (seen < need && --s > 0);
  }
  CFENCE();
}
__device__ __forceinline__ void ctl_add(LAS char* L, int w, int lane) {
  WAITL();
  if (lane == 0) __hip_atomic_fetch_add((LAS int*)(L + L_CTL + 4 * w), 1, __ATOMIC_RELAXED, __HIP_MEMORY_SCOPE_WORKGROUP);
  CFENCE();
}
__device__ __forceinline__ int prog_min(LAS char* L) {
  const u32x4 d = *(volatile LAS u32x4*)(L + L_CTL + 4 * C_PROG);
  return (int)min(min(d[0], d[1]), min(d[2], d[3]));
}
__device__ __forceinline__ void wait_released(LAS char* L, int unit) {
  int s = SPIN_CAP, v;
  do { v = __builtin_amdgcn_readfirstlane(prog_min(L)); } while (v <= unit && --s > 0);
  CFENCE();
}

__device__ __forceinline__ u32x4 layernorm8(const float (&v)[8], bool act) {
  const float c = __builtin_bit_cast(float, __builtin_amdgcn_readfirstlane(__builtin_bit_cast(int, v[0])));
  float s = 0.f, ss = 0.f;
#pragma unroll
  for (int i = 0; i < 8; ++i) { const float d = v[i] - c; s += d; ss = fmaf(d, d, ss); }
  s = wave_sum(act ? s : 0.f) * (1.f / DM);
  ss = wave_sum(act ? ss : 0.f) * (1.f / DM);
  const float var = rbf(fmaxf(fmaf(-s, s, ss), 0.f));
  const float inv = __builtin_amdgcn_rcpf(rbf(__builtin_amdgcn_sqrtf(rbf(var + 1e-5f))));
  const float mb = rbf(c + s);
  u32x4 o;
#pragma unroll
  for (int i = 0; i < 4; ++i) { const unsigned d = pack2(v[2 * i] - mb, v[2 * i + 1] - mb); o[i] = pack2(bf_lo(d) * inv, bf_hi(d) * inv); }
  return o;
}

template <int BLK, int AUX>
__device__ __forceinline__ void load_units(LAS char* L, rsrc_t r, int vo, int so0, int u0, int& published, int& fr) {
  for (int k = 0; k < NU; ++k) {
    const int u = u0 + k;
    LAS char* sl = L + (u & (NS - 1)) * SLOT;
    const int so = so0 + (k >> 1) * 32768 + (k & 1) * 8192;
    if (u >= NS && __builtin_amdgcn_readfirstlane(fr) <= u - NS) {
      WAITV(0);
      published = u; ctl_wr(L, C_LANDED, u);
      wait_released(L, u - NS);
    }
#pragma unroll
    for (int i = 0; i < 8; ++i) {
      __builtin_amdgcn_raw_ptr_buffer_load_lds(r, (LAS void*)(sl + i * BLK), 16, vo, so + i * 1024, 0, AUX);
      WAITV(NFL - 1);
      if ((i + 2 - NFL + 64) % 8 == 0) {
        const int n = u + (i + 2 - NFL + 64) / 8 - 8;
        if (n > published) { published = n; ctl_wr(L, C_LANDED, n); }
      }
      if (i == 4) fr = prog_min(L);
    }
    if (u == 1) { __builtin_amdgcn_s_barrier(); CFENCE(); }
  }
}
__device__ __forceinline__ void load_weight_head(LAS char* L, rsrc_t r, int vo, int so, int step) {
#pragma unroll
  for (int k = 0; k < 28; ++k) {
    __builtin_amdgcn_raw_ptr_buffer_load_lds(r, (LAS void*)(L + k * 1024), 16, vo, so + k * step, 0, AUX_W);
    if (k <= NFL - 2) {
      WAITV(NFL - 1);
      if ((k + 2 - NFL + 64) % 8 == 0) ctl_wr(L, C_LANDED, 2 * NU + (k + 2 - NFL + 64) / 8 - 8);
    }
  }
}
__device__ __forceinline__ void load_weight_rest(LAS char* L, rsrc_t r, int vo, int so, int step, int dst, int n4) {
#pragma nounroll
  for (int k = 0; k < n4; ++k) {
#pragma unroll
    for (int i = 0; i < 4; ++i) __builtin_amdgcn_raw_ptr_buffer_load_lds(r, (LAS void*)(L + dst + (4 * k + i) * 1024), 16, vo, so + (4 * k + i) * step, 0, AUX_W);
    WAITV(NFLW - 4);
  }
}
__device__ __forceinline__ void reload4(u32x4& a, u32x4& b, u32x4& c, u32x4& d, int vo, u32x4 srd, int so01, int so23) {
  const u64 ma = __ballot(a[3] == SENT), mb = __ballot(b[3] == SENT), mc = __ballot(c[3] == SENT), md = __ballot(d[3] == SENT);
  asm volatile("s_mov_b64 exec, %4\n\tbuffer_load_dwordx4 %0, %8, %9, %10 offen sc1\n\t"
               "s_mov_b64 exec, %5\n\tbuffer_load_dwordx4 %1, %8, %9, %10 offen offset:2048 sc1\n\t"
               "s_mov_b64 exec, %6\n\tbuffer_load_dwordx4 %2, %8, %9, %11 offen sc1\n\t"
               "s_mov_b64 exec, %7\n\tbuffer_load_dwordx4 %3, %8, %9, %11 offen offset:2048 sc1\n\t"
               "s_mov_b64 exec, -1"
               : "+v"(a), "+v"(b), "+v"(c), "+v"(d)
               : "s"(ma), "s"(mb), "s"(mc), "s"(md), "v"(vo), "s"(srd), "s"(so01), "s"(so23)
               : "memory");
}
#define LANDED8(v, k) asm volatile("s_waitcnt vmcnt(0)" : "+v"(v[k]), "+v"(v[k + 1]), "+v"(v[k + 2]), "+v"(v[k + 3]), "+v"(v[k + 4]), "+v"(v[k + 5]), "+v"(v[k + 6]), "+v"(v[k + 7]) :: "memory")
__device__ __forceinline__ bool pending16(const u32x4 (&v)[16]) {
  unsigned m = v[0][3];
#pragma unroll
  for (int k = 1; k < 16; ++k) m = max(m, v[k][3]);
  return __any(m == SENT);
}
__device__ __forceinline__ bool rearm16(u32x4 (&v)[16]) {
  unsigned m = 0u;
#pragma unroll
  for (int k = 0; k < 16; ++k) m = max(m, umax4(v[k]));
  if (!__any(m == SENT)) return false;
#pragma unroll
  for (int k = 0; k < 16; ++k) v[k][3] = SENT;
  return true;
}

__global__ void __launch_bounds__(320) fused_decode(const u16* __restrict__ x, const u16* __restrict__ Kc, const u16* __restrict__ Vc,
                                                    const u16* __restrict__ wq, const u16* __restrict__ wo, const u16* __restrict__ wg,
                                                    const u16* __restrict__ wu, const u16* __restrict__ wd, u16* __restrict__ out, char* ws) {
  __shared__ __attribute__((aligned(16))) char lds[L_END];
  LAS char* L = (LAS char*)lds;
  const int lane = threadIdx.x & 63, wave = __builtin_amdgcn_readfirstlane(threadIdx.x >> 6);
  const int bid = blockIdx.x;
  const int grp = GM ? (bid & 15) : (bid >> 4), j = GM ? (bid >> 4) : (bid & 15);
  const bool is_ffn = bid >= FFN0, is_wo = bid >= WO0 && !is_ffn;
  const int x8 = bid & 7, rk = (bid - (is_ffn ? FFN0 : WO0)) >> 3, f = 7 * x8 + rk;

  if (wave == 4) STAMP(0);
  if (wave == 4) {
    __builtin_amdgcn_s_setprio(3);
    const int vo = (lane >> 5) * 512 + (((lane & 31) ^ ((lane >> 5) << 3)) << 4);
    const int so0 = (j >> 1) * (2 << 20) + (j & 1) * 16384;
    int published = 0, fr = 0;
    load_units<KBLK, AUX_K>(L, mk(Kc + (size_t)grp * SK * DH, SK * DH * 2), vo, so0, 0, published, fr);
    STAMP(1);
    load_units<VBLK, AUX_V>(L, mk(Vc + (size_t)grp * SK * DH, SK * DH * 2), vo, so0, NU, published, fr);
    STAMP(2);
    if (is_wo || is_ffn) {
      const rsrc_t r0 = mk(is_wo ? wo : wg, is_wo ? 4096 * DM * 2 : DM * FF * 2);
      const int vo0 = is_wo ? (lane >> 3) * (DM * 2) + (lane & 7) * 16 : (lane >> 2) * (FF * 2) + (lane & 3) * 16;
      const int so0w = is_wo ? x8 * 512 * (DM * 2) + 128 * rk : 64 * f, step0 = is_wo ? 8 * DM * 2 : 16 * FF * 2;
      if (__builtin_amdgcn_readfirstlane(fr) <= 2 * NU - NS + 3) wait_released(L, 2 * NU - NS + 3);
      load_weight_head(L, r0, vo0, so0w, step0);
      wait_released(L, 2 * NU - NS + 10);
      if (is_wo) {
        load_weight_rest(L, r0, vo0, so0w + 28 * step0, step0, 28672, 9);
      } else {
        load_weight_rest(L, mk(wu, DM * FF * 2), vo0, so0w, step0, 28672, 7);
        load_weight_rest(L, mk(wd, FF * DM * 2), lane * 16, 32 * f * (DM * 2), 1024, 57344, 7);
      }
    } else {
      WAITV(16); ctl_wr(L, C_LANDED, 2 * NU - 2);
      WAITV(8); ctl_wr(L, C_LANDED, 2 * NU - 1);
    }
    WAITV(0);
    ctl_wr(L, C_LANDED, 2 * NU + 1);
    STAMP(3); CYC(31); FLUSH();
    return;
  }

  const rsrc_t rws = mk(ws, W_END);
  const int n16 = lane & 15, g4 = lane >> 4;
  const int q4 = n16 >> 2, p4 = lane & 3;
  const int row = grp * 16 + j;
  u32x2 ep = {0u, 0u};
  const u32x4 srd = {(unsigned)(size_t)ws, (unsigned)((size_t)ws >> 32) & 0xFFFFu, (unsigned)W_END, 0x00020000u};
  if (wave == 3) {
    const rsrc_t rq = mk(wq, DM * 4096 * 2);
    const int p = bid >> 3;
#pragma nounroll
    for (int u = 0; u < 14; ++u)
      __builtin_amdgcn_raw_ptr_buffer_load_lds(rq, (LAS void*)(L + L_STG + u * 1024), 16, (lane >> 1) * 8192 + (lane & 1) * 16, u * 32 * 8192 + x8 * 1024 + 32 * p, 0, AUX_Q);
    ep = __builtin_amdgcn_raw_buffer_load_b64(rws, 0, W_EPOCH, 0);
    LAS char* wq_rd = pin(L, L_STG + (8 * g4 + q4) * 32 + 8 * p4);
    LAS char* xn_rd = pin(L, L_XN + (lane & 1) * 896 + g4 * 16);
    __builtin_amdgcn_s_barrier();
    WAITV(0);
    STAMP(4);
    u32x4 wf[14];
#pragma unroll
    for (int s = 0; s < 14; ++s) wf[s] = trread2(wq_rd + s * 1024, 128);
    ctl_wait(L, C_XN, 2);
    u32x4 af[14];
#pragma unroll
    for (int s = 0; s < 14; ++s) af[s] = *(LAS u32x4*)(xn_rd + s * 64);
    __builtin_amdgcn_sched_barrier(0);
    f32x4 d = {0.f, 0.f, 0.f, 0.f};
#pragma unroll
    for (int s = 0; s < 14; ++s) d = mfma32(af[s], wf[s], d);
    const float qv = (g4 & 1) ? d[1] : d[0];
    const unsigned pr = pack2(qv, dpp<XOR1>(qv));
    if (lane < 32 && !(lane & 1))
      __builtin_amdgcn_raw_buffer_store_b64(u32x2{pr, ep[0] + 1u}, rws, ((g4 * 8 + x8) * 256 + 8 * p + (n16 >> 1)) * 8, W_Q, AUX_SC1);
    STAMP(5);
  } else if (wave > 0) {
    const int bb = wave - 1;
    const u32x4 xv = *(const u32x4*)(x + bb * DM + (lane < 56 ? lane : 55) * 8);
    ep = __builtin_amdgcn_raw_buffer_load_b64(rws, 0, W_EPOCH, 0);
    __builtin_amdgcn_s_barrier();
    float v[8];
#pragma unroll
    for (int i = 0; i < 4; ++i) { v[2 * i] = bf_lo(xv[i]); v[2 * i + 1] = bf_hi(xv[i]); }
    const u32x4 o = layernorm8(v, lane < 56);
    if (lane < 56) *(LAS u32x4*)(L + L_XN + bb * 896 + lane * 16) = o;
    ctl_add(L, C_XN, lane);
  } else {
    ep = __builtin_amdgcn_raw_buffer_load_b64(rws, 0, W_EPOCH, 0);
    ((LAS int*)(L + L_CTL))[lane] = lane < 4 ? lane : 0;
    BARRIER();
    const u32x4 s4 = {SENT, SENT, SENT, SENT};
    __builtin_amdgcn_raw_buffer_store_b128(s4, rws, lane * 16, W_PART + row * 2048, AUX_SC1);
    __builtin_amdgcn_raw_buffer_store_b128(s4, rws, lane * 16 + 1024, W_PART + row * 2048, AUX_SC1);
    if (lane == 0) __builtin_amdgcn_raw_buffer_store_b128(s4, rws, 0, W_ML + row * 16, AUX_SC1);
    if (is_wo && lane < 32) __builtin_amdgcn_raw_buffer_store_b128(s4, rws, g4 * 16384 + n16 * 16, W_Y + x8 * 2048 + rk * 256, AUX_SC1);
    if (is_ffn) {
#pragma unroll
      for (int it = 0; it < 4; ++it) {
        const int r = (lane >> 2) + 16 * it;
        if (r < NFFN) __builtin_amdgcn_raw_buffer_store_b128(s4, rws, (r * NFFN + f) * 64 + p4 * 16, W_Z, AUX_SC1);
      }
    }
    WAITV(0);
    if (lane == 0) __builtin_amdgcn_raw_buffer_store_b64(u32x2{ep[0] + 1u, ep[1]}, rws, bid * 8, W_FLAG, AUX_SC1);
  }
  CFENCE();

  const int ka = (n16 >> 1) * KBLK + (n16 & 1) * 512 + (16 * (g4 & 1) + 8 * ((g4 >> 1) ^ (n16 & 1))) * 16;
  LAS char* sc_wr = L + L_STG + wave * 2048 + n16 * 32 + g4 * 8;
  int seen = 0;
  float mx = -INFINITY;
  u32x4 qf[8];
  auto score = [&](const u32x4 (&kf)[8], int i) {
    f32x4 s0 = {0.f, 0.f, 0.f, 0.f}, s1 = {0.f, 0.f, 0.f, 0.f};
#pragma unroll
    for (int s = 0; s < 8; s += 2) { s0 = mfma32(kf[s], qf[s], s0); s1 = mfma32(kf[s + 1], qf[s + 1], s1); }
    const u32x2 sb = {pack2(s0[0] + s1[0], s0[1] + s1[1]), pack2(s0[2] + s1[2], s0[3] + s1[3])};
    mx = fmaxf(fmaxf(mx, fmaxf(bf_lo(sb[0]), bf_hi(sb[0]))), fmaxf(bf_lo(sb[1]), bf_hi(sb[1])));
    if (n16 < 2) *(LAS u32x2*)(sc_wr + i * 64) = sb;
  };
  {
    u32x4 kp[RPRE][8];
#pragma unroll
    for (int i = 0; i < RPRE; ++i) {
      const int u = 4 * i + wave;
      landed_wait(L, seen, u + 1);
#pragma unroll
      for (int s = 0; s < 8; ++s) kp[i][s] = *(LAS u32x4*)(L + u * SLOT + ka + 16 * s);
      WAITL();
      ctl_wr(L, C_PROG + wave, u + 4);
    }
    if (wave == 1) {
      u32x4 g0, g1;
      const unsigned tg = ep[0] + 1u;
      int budget = SPIN_CAP;
      do {
        CFENCE();
        g0 = __builtin_amdgcn_raw_buffer_load_b128(rws, lane * 16, W_Q + grp * 2048, AUX_SC1);
        g1 = __builtin_amdgcn_raw_buffer_load_b128(rws, lane * 16 + 1024, W_Q + grp * 2048, AUX_SC1);
      } while (__any(((g0[1] ^ tg) | (g0[3] ^ tg) | (g1[1] ^ tg) | (g1[3] ^ tg)) != 0u) && --budget > 0);
      *(LAS u32x2*)(L + L_QB + lane * 8) = u32x2{g0[0], g0[2]};
      *(LAS u32x2*)(L + L_QB + 512 + lane * 8) = u32x2{g1[0], g1[2]};
      WAITL();
      ctl_wr(L, C_Q, 1);
      STAMP(6);
    } else {
      ctl_wait(L, C_Q, 1);
    }
#pragma unroll
    for (int s = 0; s < 8; ++s) qf[s] = *(LAS u32x4*)(L + L_QB + (lane & 1) * 512 + (s + 16 * (g4 & 1) + 8 * (g4 >> 1)) * 16);
#pragma unroll
    for (int i = 0; i < RPRE; ++i) score(kp[i], i);
  }
  for (int i = RPRE; i < NU / 4; ++i) {
    const int u = 4 * i + wave;
    landed_wait(L, seen, u + 1);
    u32x4 kf[8];
#pragma unroll
    for (int s = 0; s < 8; ++s) kf[s] = *(LAS u32x4*)(L + (u & (NS - 1)) * SLOT + ka + 16 * s);
    WAITL();
    ctl_wr(L, C_PROG + wave, min(u + 4, NU));
    score(kf, i);
  }

  mx = xrow_max(mx);
  if (lane < 2) *(LAS float*)(L + L_RED + wave * 8 + lane * 4) = mx;
  ctl_add(L, C_MX, lane);
  ctl_wait(L, C_MX, 4);
  {
    const LAS float* r = (const LAS float*)(L + L_RED + (lane & 1) * 4);
    const float M = fmaxf(fmaxf(r[0], r[2]), fmaxf(r[4], r[6]));
    LAS u32x4* sp = (LAS u32x4*)(L + L_STG + wave * 2048 + lane * 32);
    float ls = 0.f;
#pragma unroll
    for (int hh = 0; hh < 2; ++hh) {
      u32x4 v = sp[hh];
#pragma unroll
      for (int i = 0; i < 4; ++i) {
        const float p0 = __builtin_amdgcn_exp2f((bf_lo(v[i]) - M) * C2), p1 = __builtin_amdgcn_exp2f((bf_hi(v[i]) - M) * C2);
        ls += p0 + p1; v[i] = pack2(p0, p1);
      }
      sp[hh] = v;
    }
    ls += dpp<XOR2>(ls); ls += dpp<ROR4>(ls); ls += dpp<ROR8>(ls); ls = xrow_sum(ls);
    if (lane < 2) *(LAS float*)(L + L_RED + 32 + wave * 8 + lane * 4) = ls;
    ctl_add(L, C_P, lane);
    ctl_wait(L, C_P, 4);
    if (wave == 0) {
      if (lane < 2) {
        const float Ls = (r[8] + r[10]) + (r[12] + r[14]);
        __builtin_amdgcn_raw_buffer_store_b64(u32x2{pub(M), pub(Ls)}, rws, lane * 8, W_ML + row * 16, AUX_SC1);
      }
      STAMP(15);
    }
  }

  f32x4 acc[4];
#pragma unroll
  for (int c = 0; c < 4; ++c) acc[c] = f32x4{0.f, 0.f, 0.f, 0.f};
  {
    const int va = (2 * g4 + (q4 >> 1)) * VBLK + (q4 & 1) * 512 + (p4 >> 1) * 16 + (p4 & 1) * 8 + ((wave & 1) ? -128 : 128) * (q4 & 1) + 128 * wave;
    LAS char* p_rd = L + L_STG + (lane & 1) * 32 + g4 * 8;
    for (int k = 0; k < NU; ++k) {
      const u32x2 pb = *(LAS u32x2*)(p_rd + (k & 3) * 2048 + (k >> 2) * 64);
      landed_wait(L, seen, NU + k + 1);
      LAS char* sl = L + (k & (NS - 1)) * SLOT + va;
      u32x2 vf[4];
#pragma unroll
      for (int c = 0; c < 4; ++c) vf[c] = trread(sl + 32 * c);
      WAITL();
      ctl_wr(L, C_PROG + wave, NU + k + 1);
#pragma unroll
      for (int c = 0; c < 4; ++c) acc[c] = mfma16(vf[c], pb, acc[c]);
      if (k == 8 && wave == 0 && (is_wo || is_ffn || bid == 0)) {
        for (int s = 0; s < SPIN_CAP; ++s) {
          const u32x4 a = __builtin_amdgcn_raw_buffer_load_b128(rws, lane * 16, W_FLAG, AUX_SC1);
          const u32x4 c = __builtin_amdgcn_raw_buffer_load_b128(rws, lane * 16 + 1024, W_FLAG, AUX_SC1);
          const unsigned lo = ep[0] + 1u, hi = ep[1];
          if (__all(a[0] == lo && a[1] == hi && a[2] == lo && a[3] == hi && c[0] == lo && c[1] == hi && c[2] == lo && c[3] == hi)) break;
          __builtin_amdgcn_s_sleep(1);
          CFENCE();
        }
        ctl_wr(L, C_VER, 1);
        if (bid == 0 && lane == 0) __builtin_amdgcn_raw_buffer_store_b64(u32x2{ep[0] + 1u, ep[1]}, rws, 0, W_EPOCH, AUX_SC1);
      }
    }
  }
  if (wave == 3) STAMP(7);
  {
    const f32x4 a = (n16 & 2) ? acc[1] : acc[0], c = (n16 & 2) ? acc[3] : acc[2], o = (n16 & 4) ? c : a;
    if (n16 < 8)
      __builtin_amdgcn_raw_buffer_store_b128(u32x4{pub(o[0]), pub(o[1]), pub(o[2]), pub(o[3])}, rws,
                                             wave * 512 + (n16 & 1) * 256 + (n16 >> 1) * 64 + g4 * 16, W_PART + row * 2048, AUX_SC1);
  }
  if (wave == 3) STAMP(8);

  if (is_wo) {
    const int hf = lane >> 5, gsel = hf * 8 + x8;
    ctl_wait(L, C_VER, 1);
    u32x2 ml;
    for (int s = 0; s < SPIN_CAP; ++s) {
      ml = __builtin_amdgcn_raw_buffer_load_b64(rws, gsel * 256 + n16 * 16 + (g4 & 1) * 8, W_ML, AUX_SC1);
      if (__all(max(ml[0], ml[1]) != SENT)) break;
      __builtin_amdgcn_s_sleep(4);
      CFENCE();
    }
    const float mj = __uint_as_float(ml[0]);
    float wt = __builtin_amdgcn_exp2f((mj - row_max(mj)) * C2);
    wt *= 1.f / row_sum(wt * __uint_as_float(ml[1]));
    landed_wait(L, seen, 2 * NU + 1);
    u32x4 wf[16];
#pragma unroll
    for (int s = 0; s < 16; ++s) wf[s] = trread2(L + (32 * s + 8 * g4 + q4) * 128 + wave * 32 + 8 * p4, 512);
    if (wave == 0) STAMP(16);
    for (int s = 0; s < SPIN_CAP; ++s) {
      const unsigned probe = __builtin_amdgcn_raw_buffer_load_b32(rws, gsel * 32768 + n16 * 2048 + wave * 512 + (g4 & 1) * 256 + 252, W_PART, AUX_SC1);
      if (__builtin_popcountll(__ballot(probe == SENT)) <= 16) break;
      __builtin_amdgcn_s_sleep(2);
      CFENCE();
    }
    float wb[16];
#define BC_(k) wb[k] = dpp<NEWBC + k>(wt);
    BC_(0) BC_(1) BC_(2) BC_(3) BC_(4) BC_(5) BC_(6) BC_(7) BC_(8) BC_(9) BC_(10) BC_(11) BC_(12) BC_(13) BC_(14) BC_(15)
#undef BC_
    LAS char* ob_wr = pin(L, L_OB + hf * 1024 + (g4 & 1) * 512 + wave * 128 + n16 * 8);
    LAS char* ob_rd = pin(L, L_OB + (lane & 1) * 1024 + g4 * 16);
    int y_off = g4 * 16384 + wave * 64 + n16 * 4;
    asm volatile("" : "+v"(y_off));
    u32x4 pv[16];
#pragma unroll
    for (int k = 0; k < 16; ++k) pv[k] = u32x4{0u, 0u, 0u, SENT};
    f32x4 o0, o1;
    const int pvo = gsel * 32768 + wave * 512 + (lane & 31) * 16;
    int budget = SPIN_CAP;
    bool again;
    do {
      do {
#pragma unroll
        for (int k = 0; k < 16; k += 4) reload4(pv[k], pv[k + 1], pv[k + 2], pv[k + 3], pvo, srd, W_PART + k * 2048, W_PART + k * 2048 + 4096);
        LANDED8(pv, 0); LANDED8(pv, 8);
      } while (pending16(pv) && --budget > 0);
      o0 = wb[0] * __builtin_bit_cast(f32x4, pv[0]); o1 = wb[1] * __builtin_bit_cast(f32x4, pv[1]);
#pragma unroll
      for (int k = 2; k < 16; k += 2) { o0 += wb[k] * __builtin_bit_cast(f32x4, pv[k]); o1 += wb[k + 1] * __builtin_bit_cast(f32x4, pv[k + 1]); }
      o0 += o1;
      const float chk = (o0[0] + o0[1]) + (o0[2] + o0[3]);
      again = false;
      if (__builtin_expect(__any(chk != chk), 0)) again = rearm16(pv) && --budget > 0;
    } while (__builtin_expect(again, 0));
    if (wave == 0) STAMP(9);
    *(LAS u32x2*)ob_wr = u32x2{pack2(o0[0], o0[1]), pack2(o0[2], o0[3])};
    BARRIER();
    f32x4 d0 = {0.f, 0.f, 0.f, 0.f}, d1 = d0;
    u32x4 af[16];
#pragma unroll
    for (int s = 0; s < 16; ++s) af[s] = *(LAS u32x4*)(ob_rd + s * 64);
    __builtin_amdgcn_sched_barrier(0);
#pragma unroll
    for (int s = 0; s < 16; s += 2) { d0 = mfma32(af[s], wf[s], d0); d1 = mfma32(af[s + 1], wf[s + 1], d1); }
    if (lane < 32)
      __builtin_amdgcn_raw_buffer_store_b32(pub((g4 & 1) ? d0[1] + d1[1] : d0[0] + d1[0]), rws, y_off, W_Y + x8 * 2048 + rk * 256, AUX_SC1);
    if (wave == 0) STAMP(10);
  }
  if (!is_ffn) { FLUSH(); return; }

  const int ln = lane < 56 ? lane : 55;
  u32x2 xa = {0u, 0u}, xb = {0u, 0u};
  if (wave < 2) { xa = *(const u32x2*)(x + wave * DM + ln * 4); xb = *(const u32x2*)(x + wave * DM + 224 + ln * 4); }
  ctl_wait(L, C_VER, 1);
  landed_wait(L, seen, 2 * NU + 1);
  u32x4 gf[14], df[7];
#pragma unroll
  for (int s = 0; s < 14; ++s) gf[s] = trread2(L + (p4 >> 1) * 28672 + (32 * s + 8 * g4 + q4) * 64 + wave * 16 + 8 * (p4 & 1), 256);
#pragma unroll
  for (int i = 0; i < 7; ++i) df[i] = trread2(L + 57344 + (8 * g4 + q4) * 896 + (7 * wave + i) * 32 + 8 * p4, 4 * 896);
  LAS char* xn_rd = pin(L, L_XN + (lane & 1) * 896 + g4 * 16);
  LAS char* xn_wr = pin(L, L_XN + wave * 896 + lane * 8);
  LAS char* hb_wr = pin(L, L_HB + wave * 1792 + lane * 16);
  LAS char* gb_wr = pin(L, L_GB + g4 * 64 + (8 * wave + n16) * 2);
  LAS char* gb_rd = pin(L, L_GB + (lane & 1) * 64 + g4 * 16);
  int z_off = (((g4 & 1) * 28 + 7 * wave + (g4 >> 1)) * NFFN + f) * 64 + n16 * 4;
  asm volatile("" : "+v"(z_off));
  if (wave == 0) STAMP(17);
  if (wave < 2) {
    for (int s = 0; s < SPIN_CAP; ++s) {
      const unsigned probe = __builtin_amdgcn_raw_buffer_load_b32(rws, (ln / 7) * 2048 + (ln % 7) * 256 + 252, W_Y + wave * 16384, AUX_SC1);
      if (__builtin_popcountll(__ballot(probe == SENT)) <= 21) break;
      __builtin_amdgcn_s_sleep(2);
      CFENCE();
    }
    u32x4 yv[16];
#pragma unroll
    for (int k = 0; k < 16; ++k) yv[k] = u32x4{0u, 0u, 0u, SENT};
    f32x4 sa, sb;
    int budget = SPIN_CAP;
    bool again;
    do {
      do {
#pragma unroll
        for (int k = 0; k < 8; k += 4) {
          reload4(yv[k], yv[k + 1], yv[k + 2], yv[k + 3], ln * 16, srd, W_Y + wave * 16384 + k * 2048, W_Y + wave * 16384 + k * 2048 + 4096);
          reload4(yv[8 + k], yv[9 + k], yv[10 + k], yv[11 + k], ln * 16 + 896, srd, W_Y + wave * 16384 + k * 2048, W_Y + wave * 16384 + k * 2048 + 4096);
        }
        LANDED8(yv, 0); LANDED8(yv, 8);
      } while (pending16(yv) && --budget > 0);
      sa = __builtin_bit_cast(f32x4, yv[0]); sb = __builtin_bit_cast(f32x4, yv[8]);
#pragma unroll
      for (int k = 1; k < 8; ++k) { sa += __builtin_bit_cast(f32x4, yv[k]); sb += __builtin_bit_cast(f32x4, yv[8 + k]); }
      const f32x4 t = sa + sb;
      const float chk = (t[0] + t[1]) + (t[2] + t[3]);
      again = false;
      if (__builtin_expect(__any(chk != chk), 0)) again = rearm16(yv) && --budget > 0;
    } while (__builtin_expect(again, 0));
    if (wave == 0) STAMP(11);
    float v[8];
#pragma unroll
    for (int i = 0; i < 2; ++i) {
      const unsigned ra = pack2(sa[2 * i], sa[2 * i + 1]), rb = pack2(sb[2 * i], sb[2 * i + 1]);
      const unsigned ha = pack2(bf_lo(xa[i]) + bf_lo(ra), bf_hi(xa[i]) + bf_hi(ra)), hb = pack2(bf_lo(xb[i]) + bf_lo(rb), bf_hi(xb[i]) + bf_hi(rb));
      v[2 * i] = bf_lo(ha); v[2 * i + 1] = bf_hi(ha); v[4 + 2 * i] = bf_lo(hb); v[5 + 2 * i] = bf_hi(hb);
    }
    const u32x4 o = layernorm8(v, lane < 56);
    if (lane < 56) {
      *(LAS u32x2*)xn_wr = u32x2{o[0], o[1]};
      *(LAS u32x2*)(xn_wr + 448) = u32x2{o[2], o[3]};
      *(LAS f32x4*)hb_wr = f32x4{v[0], v[1], v[2], v[3]};
      *(LAS f32x4*)(hb_wr + 896) = f32x4{v[4], v[5], v[6], v[7]};
    }
  }
  BARRIER();
  {
    f32x4 d0 = {0.f, 0.f, 0.f, 0.f}, d1 = d0;
    u32x4 af[14];
#pragma unroll
    for (int s = 0; s < 14; ++s) af[s] = *(LAS u32x4*)(xn_rd + s * 64);
    __builtin_amdgcn_sched_barrier(0);
#pragma unroll
    for (int s = 0; s < 14; s += 2) { d0 = mfma32(af[s], gf[s], d0); d1 = mfma32(af[s + 1], gf[s + 1], d1); }
    const float a = rbf((g4 & 1) ? d0[1] + d1[1] : d0[0] + d1[0]), u = dpp<ROR8>(a);
    const float z = 0.7978845608028654f * (a + 0.044715f * a * a * a);
    const float ge = rbf(a * __builtin_amdgcn_rcpf(1.f + __builtin_amdgcn_exp2f(-2.f * LOG2E * z)));
    if (lane < 32 && n16 < 8) *(LAS u16*)gb_wr = (u16)pack2(ge * u, 0.f);
  }
  BARRIER();
  {
    const u32x4 a = *(LAS u32x4*)gb_rd;
    f32x4 d[8];
#pragma unroll
    for (int i = 0; i < 7; ++i) d[i] = mfma32(a, df[i], f32x4{0.f, 0.f, 0.f, 0.f});
    d[7] = d[6];
#pragma unroll
    for (int i = 0; i < 4; ++i) {
      const float v0 = (g4 & 1) ? d[2 * i][1] : d[2 * i][0], v1 = (g4 & 1) ? d[2 * i + 1][1] : d[2 * i + 1][0];
      if (i < 3 || lane < 32) __builtin_amdgcn_raw_buffer_store_b32(pub((g4 >> 1) ? v1 : v0), rws, z_off, W_Z + 2 * i * NFFN * 64, AUX_SC1);
    }
  }
  if (wave != 0) { FLUSH(); return; }
  STAMP(12);

  {
    const int ob = f / 28, oc = (f % 28) * 16 + p4 * 4;
    const f32x4 hv = *(LAS f32x4*)(L + L_HB + ob * 1792 + oc * 4);
    u32x4 zv[4];
    int budget = SPIN_CAP;
    unsigned mxx;
    do {
      CFENCE();
#pragma unroll
      for (int k = 0; k < 4; ++k) zv[k] = __builtin_amdgcn_raw_buffer_load_b128(rws, (k < 3 ? lane : (lane & 31)) * 16 + k * 1024, W_Z + f * (NFFN * 64), AUX_SC1);
      mxx = max(max(umax4(zv[0]), umax4(zv[1])), max(umax4(zv[2]), umax4(zv[3])));
    } while (__any(mxx == SENT) && --budget > 0);
    STAMP(13);
    f32x4 sum = (__builtin_bit_cast(f32x4, zv[0]) + __builtin_bit_cast(f32x4, zv[1])) + __builtin_bit_cast(f32x4, zv[2]);
    if (lane < 32) sum += __builtin_bit_cast(f32x4, zv[3]);
    float r[4];
#pragma unroll
    for (int i = 0; i < 4; ++i) { float t = sum[i]; t += dpp<ROR4>(t); t += dpp<ROR8>(t); r[i] = hv[i] + rbf(xrow_sum(t)); }
    if (lane < 4) *(u32x2*)(out + ob * DM + oc) = u32x2{pack2(r[0], r[1]), pack2(r[2], r[3])};
    STAMP(14); FLUSH();
    if (bid == FFN0) {
      WAITL();
      const u64 a = *(volatile LAS u64*)(L + L_STAMP), c = *(volatile LAS u64*)(L + L_STAMP + DIAG * 8);
      u64 d = (c - a) * DIAGK + DIAGC; if (d > 100000) d = 100000;
      const u64 t = __builtin_amdgcn_s_memrealtime();
      while (__builtin_amdgcn_s_memrealtime() - t < d) __builtin_amdgcn_s_sleep(1);
    }
  }
}

extern "C" void kernel_launch(void* const* d_in, const int* in_sizes, int n_in, void* d_out, int out_size, void* d_ws, size_t ws_size,
                              hipStream_t stream) {
  fused_decode<<<256, 320, 0, stream>>>((const u16*)d_in[0], (const u16*)d_in[1], (const u16*)d_in[2], (const u16*)d_in[3], (const u16*)d_in[4],
                                        (const u16*)d_in[6], (const u16*)d_in[7], (const u16*)d_in[8], (u16*)d_out, (char*)d_ws);
}
```

```cpp
#include <hip/hip_runtime.h>
#include <hip/hip_bf16.h>
#include <stdint.h>

#define LAS __attribute__((address_space(3)))
typedef unsigned short u16;
typedef unsigned long long u64;
typedef unsigned u32x2 __attribute__((ext_vector_type(2)));
typedef unsigned u32x4 __attribute__((ext_vector_type(4)));
typedef float f32x2 __attribute__((ext_vector_type(2)));
typedef float f32x4 __attribute__((ext_vector_type(4)));
typedef short s16x4 __attribute__((ext_vector_type(4)));
typedef __bf16 bf16x2 __attribute__((ext_vector_type(2)));
typedef __bf16 bf16x8 __attribute__((ext_vector_type(8)));
typedef __amdgpu_buffer_rsrc_t rsrc_t;

#define NFL 20
#define NFLW 40
#define AUX_W 2
#define AUX_Q 2
#define GM 0
#define RPRE 3

constexpr int DM = 448, SK = 32768, DH = 256, FF = 1792;
constexpr int NU = 128;
constexpr int NS = 16, KBLK = 1040, VBLK = 1056, SLOT = 8 * VBLK;
constexpr int L_STG = NS * SLOT, L_XN = L_STG + 14336, L_QB = L_XN + 1792, L_HB = L_QB + 1024, L_GB = L_HB + 3584,
              L_OB = L_GB + 128, L_RED = L_OB + 2048, L_CTL = L_RED + 64, L_STAMP = L_CTL + 256, L_END = L_STAMP + 1280;
constexpr int C_PROG = 0, C_LANDED = 4, C_XN = 5, C_Q = 6, C_VER = 7, C_MX = 8, C_P = 9;
constexpr int W_EPOCH = 0, W_FLAG = 4096, W_Q = 8192, W_ML = 40960, W_Y = 49152, W_PART = 131072, W_Z = 655360,
              W_END = 1 << 20;
constexpr int WO0 = 144, FFN0 = 200, NFFN = 56;
constexpr unsigned SENT = 0xFFFFFFFFu;
constexpr int AUX_SC1 = 16, AUX_NT = 2;
constexpr int SPIN_CAP = 1 << 20;
constexpr float LOG2E = 1.4426950408889634f, C2 = 0.0625f * LOG2E;

#define WAITV(n) asm volatile("s_waitcnt vmcnt(%0)" :: "i"(n) : "memory")
#define WAITL() asm volatile("s_waitcnt lgkmcnt(0)" ::: "memory")
#define CFENCE() asm volatile("" ::: "memory")
#define BARRIER() do { WAITL(); __builtin_amdgcn_s_barrier(); CFENCE(); } while (0)

#define STAMP(k) do { } while (0)
#define CYC(k) do { } while (0)
#define FLUSH() do { } while (0)

__device__ __forceinline__ float bf_lo(unsigned v) { return __uint_as_float(v << 16); }
__device__ __forceinline__ float bf_hi(unsigned v) { return __uint_as_float(v & 0xffff0000u); }
__device__ __forceinline__ unsigned pack2(float lo, float hi) { return __builtin_bit_cast(unsigned, __builtin_convertvector(f32x2{lo, hi}, bf16x2)); }
__device__ __forceinline__ float rbf(float f) { return bf_lo(pack2(f, f)); }
__device__ __forceinline__ unsigned pub(float f) { unsigned u = __float_as_uint(f); return u == SENT ? 0x7FC00000u : u; }
template <int CTRL> __device__ __forceinline__ float dpp(float x) {
  return __builtin_bit_cast(float, __builtin_amdgcn_mov_dpp(__builtin_bit_cast(int, x), CTRL, 0xf, 0xf, true));
}
constexpr int XOR1 = 0xB1, XOR2 = 0x4E, XOR7 = 0x141, ROR8 = 0x128, ROR4 = 0x124, NEWBC = 0x150;
__device__ __forceinline__ float xrow_max(float x) {
  auto s = __builtin_amdgcn_permlane16_swap(__float_as_uint(x), __float_as_uint(x), false, false);
  x = fmaxf(__uint_as_float(s[0]), __uint_as_float(s[1]));
  auto t = __builtin_amdgcn_permlane32_swap(__float_as_uint(x), __float_as_uint(x), false, false);
  return fmaxf(__uint_as_float(t[0]), __uint_as_float(t[1]));
}
__device__ __forceinline__ float xrow_sum(float x) {
  auto s = __builtin_amdgcn_permlane16_swap(__float_as_uint(x), __float_as_uint(x), false, false);
  x = __uint_as_float(s[0]) + __uint_as_float(s[1]);
  auto t = __builtin_amdgcn_permlane32_swap(__float_as_uint(x), __float_as_uint(x), false, false);
  return __uint_as_float(t[0]) + __uint_as_float(t[1]);
}
__device__ __forceinline__ float row_sum(float x) { x += dpp<XOR1>(x); x += dpp<XOR2>(x); x += dpp<XOR7>(x); x += dpp<ROR8>(x); return x; }
__device__ __forceinline__ float row_max(float x) {
  x = fmaxf(x, dpp<XOR1>(x)); x = fmaxf(x, dpp<XOR2>(x)); x = fmaxf(x, dpp<XOR7>(x)); x = fmaxf(x, dpp<ROR8>(x)); return x;
}
__device__ __forceinline__ float wave_sum(float x) { return xrow_sum(row_sum(x)); }
__device__ __forceinline__ unsigned umax4(u32x4 v) { return max(max(v[0], v[1]), max(v[2], v[3])); }

__device__ __forceinline__ rsrc_t mk(const void* p, unsigned bytes) { return __builtin_amdgcn_make_buffer_rsrc((void*)p, 0, bytes, 0x00020000); }
__device__ __forceinline__ f32x4 mfma32(u32x4 a, u32x4 b, f32x4 c) {
  return __builtin_amdgcn_mfma_f32_16x16x32_bf16(__builtin_bit_cast(bf16x8, a), __builtin_bit_cast(bf16x8, b), c, 0, 0, 0);
}
__device__ __forceinline__ f32x4 mfma16(u32x2 a, u32x2 b, f32x4 c) {
  return __builtin_amdgcn_mfma_f32_16x16x16bf16_1k(__builtin_bit_cast(s16x4, a), __builtin_bit_cast(s16x4, b), c, 0, 0, 0);
}
__device__ __forceinline__ u32x2 trread(LAS char* p) {
  return __builtin_bit_cast(u32x2, __builtin_amdgcn_ds_read_tr16_b64_v4i16((LAS s16x4*)p));
}
__device__ __forceinline__ u32x4 trread2(LAS char* p, int stride4) {
  const u32x2 a = trread(p), b = trread(p + stride4);
  return u32x4{a[0], a[1], b[0], b[1]};
}
__device__ __forceinline__ LAS char* pin(LAS char* L, int off) { asm volatile("" : "+v"(off)); return L + off; }
__device__ __forceinline__ int ctl_rd(LAS char* L, int w) { return __builtin_amdgcn_readfirstlane(*(volatile LAS int*)(L + L_CTL + 4 * w)); }
__device__ __forceinline__ void ctl_wr(LAS char* L, int w, int v) { *(volatile LAS int*)(L + L_CTL + 4 * w) = v; }
__device__ __forceinline__ void ctl_wait(LAS char* L, int w, int need) {
  int s = SPIN_CAP;
  while (ctl_rd(L, w) < need && --s > 0) __builtin_amdgcn_s_sleep(1);
  CFENCE();
}
__device__ __forceinline__ void landed_wait(LAS char* L, int& seen, int need) {
  if (seen < need) {
    int s = SPIN_CAP;
    do { seen = ctl_rd(L, C_LANDED); } while (seen < need && --s > 0);
  }
  CFENCE();
}
__device__ __forceinline__ void ctl_add(LAS char* L, int w, int lane) {
  WAITL();
  if (lane == 0) __hip_atomic_fetch_add((LAS int*)(L + L_CTL + 4 * w), 1, __ATOMIC_RELAXED, __HIP_MEMORY_SCOPE_WORKGROUP);
  CFENCE();
}
__device__ __forceinline__ int prog_min(LAS char* L) {
  const u32x4 d = *(volatile LAS u32x4*)(L + L_CTL + 4 * C_PROG);
  return (int)min(min(d[0], d[1]), min(d[2], d[3]));
}
__device__ __forceinline__ void wait_released(LAS char* L, int unit) {
  int s = SPIN_CAP, v;
  do { v = __builtin_amdgcn_readfirstlane(prog_min(L)); } while (v <= unit && --s > 0);
  CFENCE();
}

__device__ __forceinline__ u32x4 layernorm8(const float (&v)[8], bool act) {
  const float c = __builtin_bit_cast(float, __builtin_amdgcn_readfirstlane(__builtin_bit_cast(int, v[0])));
  float s = 0.f, ss = 0.f;
#pragma unroll
  for (int i = 0; i < 8; ++i) { const float d = v[i] - c; s += d; ss = fmaf(d, d, ss); }
  s = wave_sum(act ? s : 0.f) * (1.f / DM);
  ss = wave_sum(act ? ss : 0.f) * (1.f / DM);
  const float var = rbf(fmaxf(fmaf(-s, s, ss), 0.f));
  const float inv = __builtin_amdgcn_rcpf(rbf(__builtin_amdgcn_sqrtf(rbf(var + 1e-5f))));
  const float mb = rbf(c + s);
  u32x4 o;
#pragma unroll
  for (int i = 0; i < 4; ++i) { const unsigned d = pack2(v[2 * i] - mb, v[2 * i + 1] - mb); o[i] = pack2(bf_lo(d) * inv, bf_hi(d) * inv); }
  return o;
}

template <int BLK>
__device__ __forceinline__ void load_units(LAS char* L, rsrc_t r, int vo, int so0, int u0, int& published, int& fr) {
  for (int k = 0; k < NU; ++k) {
    const int u = u0 + k;
    LAS char* sl = L + (u & (NS - 1)) * SLOT;
    const int so = so0 + (k >> 1) * 32768 + (k & 1) * 8192;
    if (u >= NS && __builtin_amdgcn_readfirstlane(fr) <= u - NS) {
      WAITV(0);
      published = u; ctl_wr(L, C_LANDED, u);
      wait_released(L, u - NS);
    }
#pragma unroll
    for (int i = 0; i < 8; ++i) {
      __builtin_amdgcn_raw_ptr_buffer_load_lds(r, (LAS void*)(sl + i * BLK), 16, vo, so + i * 1024, 0, AUX_NT);
      WAITV(NFL - 1);
      if ((i + 2 - NFL + 64) % 8 == 0) {
        const int n = u + (i + 2 - NFL + 64) / 8 - 8;
        if (n > published) { published = n; ctl_wr(L, C_LANDED, n); }
      }
      if (i == 4) fr = prog_min(L);
    }
    if (u == 1) { __builtin_amdgcn_s_barrier(); CFENCE(); }
  }
}
__device__ __forceinline__ void load_weight_head(LAS char* L, rsrc_t r, int vo, int so, int step) {
#pragma unroll
  for (int k = 0; k < 28; ++k) {
    __builtin_amdgcn_raw_ptr_buffer_load_lds(r, (LAS void*)(L + k * 1024), 16, vo, so + k * step, 0, AUX_W);
    if (k <= NFL - 2) {
      WAITV(NFL - 1);
      if ((k + 2 - NFL + 64) % 8 == 0) ctl_wr(L, C_LANDED, 2 * NU + (k + 2 - NFL + 64) / 8 - 8);
    }
  }
}
__device__ __forceinline__ void load_weight_rest(LAS char* L, rsrc_t r, int vo, int so, int step, int dst, int n4) {
#pragma nounroll
  for (int k = 0; k < n4; ++k) {
#pragma unroll
    for (int i = 0; i < 4; ++i) __builtin_amdgcn_raw_ptr_buffer_load_lds(r, (LAS void*)(L + dst + (4 * k + i) * 1024), 16, vo, so + (4 * k + i) * step, 0, AUX_W);
    WAITV(NFLW - 4);
  }
}
__device__ __forceinline__ void reload4(u32x4& a, u32x4& b, u32x4& c, u32x4& d, int vo, u32x4 srd, int so01, int so23) {
  const u64 ma = __ballot(a[3] == SENT), mb = __ballot(b[3] == SENT), mc = __ballot(c[3] == SENT), md = __ballot(d[3] == SENT);
  asm volatile("s_mov_b64 exec, %4\n\tbuffer_load_dwordx4 %0, %8, %9, %10 offen sc1\n\t"
               "s_mov_b64 exec, %5\n\tbuffer_load_dwordx4 %1, %8, %9, %10 offen offset:2048 sc1\n\t"
               "s_mov_b64 exec, %6\n\tbuffer_load_dwordx4 %2, %8, %9, %11 offen sc1\n\t"
               "s_mov_b64 exec, %7\n\tbuffer_load_dwordx4 %3, %8, %9, %11 offen offset:2048 sc1\n\t"
               "s_mov_b64 exec, -1"
               : "+v"(a), "+v"(b), "+v"(c), "+v"(d)
               : "s"(ma), "s"(mb), "s"(mc), "s"(md), "v"(vo), "s"(srd), "s"(so01), "s"(so23)
               : "memory");
}
#define LANDED8(v, k) asm volatile("s_waitcnt vmcnt(0)" : "+v"(v[k]), "+v"(v[k + 1]), "+v"(v[k + 2]), "+v"(v[k + 3]), "+v"(v[k + 4]), "+v"(v[k + 5]), "+v"(v[k + 6]), "+v"(v[k + 7]) :: "memory")
__device__ __forceinline__ bool pending16(const u32x4 (&v)[16]) {
  unsigned m = v[0][3];
#pragma unroll
  for (int k = 1; k < 16; ++k) m = max(m, v[k][3]);
  return __any(m == SENT);
}
__device__ __forceinline__ bool rearm16(u32x4 (&v)[16]) {
  unsigned m = 0u;
#pragma unroll
  for (int k = 0; k < 16; ++k) m = max(m, umax4(v[k]));
  if (!__any(m == SENT)) return false;
#pragma unroll
  for (int k = 0; k < 16; ++k) v[k][3] = SENT;
  return true;
}

__global__ void __launch_bounds__(320) fused_decode(const u16* __restrict__ x, const u16* __restrict__ Kc, const u16* __restrict__ Vc,
                                                    const u16* __restrict__ wq, const u16* __restrict__ wo, const u16* __restrict__ wg,
                                                    const u16* __restrict__ wu, const u16* __restrict__ wd, u16* __restrict__ out, char* ws) {
  __shared__ __attribute__((aligned(16))) char lds[L_END];
  LAS char* L = (LAS char*)lds;
  const int lane = threadIdx.x & 63, wave = __builtin_amdgcn_readfirstlane(threadIdx.x >> 6);
  const int bid = blockIdx.x;
  const int grp = GM ? (bid & 15) : (bid >> 4), j = GM ? (bid >> 4) : (bid & 15);
  const bool is_ffn = bid >= FFN0, is_wo = bid >= WO0 && !is_ffn;
  const int x8 = bid & 7, rk = (bid - (is_ffn ? FFN0 : WO0)) >> 3, f = 7 * x8 + rk;

  if (wave == 4) {
    __builtin_amdgcn_s_setprio(3);
    const int vo = (lane >> 5) * 512 + (((lane & 31) ^ ((lane >> 5) << 3)) << 4);
    const int so0 = (j >> 1) * (2 << 20) + (j & 1) * 16384;
    int published = 0, fr = 0;
    load_units<KBLK>(L, mk(Kc + (size_t)grp * SK * DH, SK * DH * 2), vo, so0, 0, published, fr);
    STAMP(1);
    load_units<VBLK>(L, mk(Vc + (size_t)grp * SK * DH, SK * DH * 2), vo, so0, NU, published, fr);
    STAMP(2);
    if (is_wo || is_ffn) {
      const rsrc_t r0 = mk(is_wo ? wo : wg, is_wo ? 4096 * DM * 2 : DM * FF * 2);
      const int vo0 = is_wo ? (lane >> 3) * (DM * 2) + (lane & 7) * 16 : (lane >> 2) * (FF * 2) + (lane & 3) * 16;
      const int so0w = is_wo ? x8 * 512 * (DM * 2) + 128 * rk : 64 * f, step0 = is_wo ? 8 * DM * 2 : 16 * FF * 2;
      if (__builtin_amdgcn_readfirstlane(fr) <= 2 * NU - NS + 3) wait_released(L, 2 * NU - NS + 3);
      load_weight_head(L, r0, vo0, so0w, step0);
      wait_released(L, 2 * NU - NS + 10);
      if (is_wo) {
        load_weight_rest(L, r0, vo0, so0w + 28 * step0, step0, 28672, 9);
      } else {
        load_weight_rest(L, mk(wu, DM * FF * 2), vo0, so0w, step0, 28672, 7);
        load_weight_rest(L, mk(wd, FF * DM * 2), lane * 16, 32 * f * (DM * 2), 1024, 57344, 7);
      }
    } else {
      WAITV(16); ctl_wr(L, C_LANDED, 2 * NU - 2);
      WAITV(8); ctl_wr(L, C_LANDED, 2 * NU - 1);
    }
    WAITV(0);
    ctl_wr(L, C_LANDED, 2 * NU + 1);
    STAMP(3); CYC(31); FLUSH();
    return;
  }

  const rsrc_t rws = mk(ws, W_END);
  const int n16 = lane & 15, g4 = lane >> 4;
  const int q4 = n16 >> 2, p4 = lane & 3;
  const int row = grp * 16 + j;
  u32x2 ep = {0u, 0u};
  const u32x4 srd = {(unsigned)(size_t)ws, (unsigned)((size_t)ws >> 32) & 0xFFFFu, (unsigned)W_END, 0x00020000u};
  if (wave == 3) {
    const rsrc_t rq = mk(wq, DM * 4096 * 2);
    const int p = bid >> 3;
#pragma nounroll
    for (int u = 0; u < 14; ++u)
      __builtin_amdgcn_raw_ptr_buffer_load_lds(rq, (LAS void*)(L + L_STG + u * 1024), 16, (lane >> 1) * 8192 + (lane & 1) * 16, u * 32 * 8192 + x8 * 1024 + 32 * p, 0, AUX_Q);
    ep = __builtin_amdgcn_raw_buffer_load_b64(rws, 0, W_EPOCH, 0);
    LAS char* wq_rd = pin(L, L_STG + (8 * g4 + q4) * 32 + 8 * p4);
    LAS char* xn_rd = pin(L, L_XN + (lane & 1) * 896 + g4 * 16);
    __builtin_amdgcn_s_barrier();
    WAITV(0);
    STAMP(4);
    u32x4 wf[14];
#pragma unroll
    for (int s = 0; s < 14; ++s) wf[s] = trread2(wq_rd + s * 1024, 128);
    ctl_wait(L, C_XN, 2);
    u32x4 af[14];
#pragma unroll
    for (int s = 0; s < 14; ++s) af[s] = *(LAS u32x4*)(xn_rd + s * 64);
    __builtin_amdgcn_sched_barrier(0);
    f32x4 d = {0.f, 0.f, 0.f, 0.f};
#pragma unroll
    for (int s = 0; s < 14; ++s) d = mfma32(af[s], wf[s], d);
    const float qv = (g4 & 1) ? d[1] : d[0];
    const unsigned pr = pack2(qv, dpp<XOR1>(qv));
    if (lane < 32 && !(lane & 1))
      __builtin_amdgcn_raw_buffer_store_b64(u32x2{pr, ep[0] + 1u}, rws, ((g4 * 8 + x8) * 256 + 8 * p + (n16 >> 1)) * 8, W_Q, AUX_SC1);
    STAMP(5);
  } else if (wave > 0) {
    const int bb = wave - 1;
    const u32x4 xv = *(const u32x4*)(x + bb * DM + (lane < 56 ? lane : 55) * 8);
    ep = __builtin_amdgcn_raw_buffer_load_b64(rws, 0, W_EPOCH, 0);
    __builtin_amdgcn_s_barrier();
    float v[8];
#pragma unroll
    for (int i = 0; i < 4; ++i) { v[2 * i] = bf_lo(xv[i]); v[2 * i + 1] = bf_hi(xv[i]); }
    const u32x4 o = layernorm8(v, lane < 56);
    if (lane < 56) *(LAS u32x4*)(L + L_XN + bb * 896 + lane * 16) = o;
    ctl_add(L, C_XN, lane);
  } else {
    ep = __builtin_amdgcn_raw_buffer_load_b64(rws, 0, W_EPOCH, 0);
    ((LAS int*)(L + L_CTL))[lane] = lane < 4 ? lane : 0;
    BARRIER();
    const u32x4 s4 = {SENT, SENT, SENT, SENT};
    __builtin_amdgcn_raw_buffer_store_b128(s4, rws, lane * 16, W_PART + row * 2048, AUX_SC1);
    __builtin_amdgcn_raw_buffer_store_b128(s4, rws, lane * 16 + 1024, W_PART + row * 2048, AUX_SC1);
    if (lane == 0) __builtin_amdgcn_raw_buffer_store_b128(s4, rws, 0, W_ML + row * 16, AUX_SC1);
    if (is_wo && lane < 32) __builtin_amdgcn_raw_buffer_store_b128(s4, rws, g4 * 16384 + n16 * 16, W_Y + x8 * 2048 + rk * 256, AUX_SC1);
    if (is_ffn) {
#pragma unroll
      for (int it = 0; it < 4; ++it) {
        const int r = (lane >> 2) + 16 * it;
        if (r < NFFN) __builtin_amdgcn_raw_buffer_store_b128(s4, rws, (r * NFFN + f) * 64 + p4 * 16, W_Z, AUX_SC1);
      }
    }
    WAITV(0);
    if (lane == 0) __builtin_amdgcn_raw_buffer_store_b64(u32x2{ep[0] + 1u, ep[1]}, rws, bid * 8, W_FLAG, AUX_SC1);
  }
  CFENCE();

  const int ka = (n16 >> 1) * KBLK + (n16 & 1) * 512 + (16 * (g4 & 1) + 8 * ((g4 >> 1) ^ (n16 & 1))) * 16;
  LAS char* sc_wr = L + L_STG + wave * 2048 + n16 * 32 + g4 * 8;
  int seen = 0;
  float mx = -INFINITY;
  u32x4 qf[8];
  auto score = [&](const u32x4 (&kf)[8], int i) {
    f32x4 s0 = {0.f, 0.f, 0.f, 0.f}, s1 = {0.f, 0.f, 0.f, 0.f};
#pragma unroll
    for (int s = 0; s < 8; s += 2) { s0 = mfma32(kf[s], qf[s], s0); s1 = mfma32(kf[s + 1], qf[s + 1], s1); }
    const u32x2 sb = {pack2(s0[0] + s1[0], s0[1] + s1[1]), pack2(s0[2] + s1[2], s0[3] + s1[3])};
    mx = fmaxf(fmaxf(mx, fmaxf(bf_lo(sb[0]), bf_hi(sb[0]))), fmaxf(bf_lo(sb[1]), bf_hi(sb[1])));
    if (n16 < 2) *(LAS u32x2*)(sc_wr + i * 64) = sb;
  };
  {
    u32x4 kp[RPRE][8];
#pragma unroll
    for (int i = 0; i < RPRE; ++i) {
      const int u = 4 * i + wave;
      landed_wait(L, seen, u + 1);
#pragma unroll
      for (int s = 0; s < 8; ++s) kp[i][s] = *(LAS u32x4*)(L + u * SLOT + ka + 16 * s);
      WAITL();
      ctl_wr(L, C_PROG + wave, u + 4);
    }
    if (wave == 1) {
      u32x4 g0, g1;
      const unsigned tg = ep[0] + 1u;
      int budget = SPIN_CAP;
      do {
        CFENCE();
        g0 = __builtin_amdgcn_raw_buffer_load_b128(rws, lane * 16, W_Q + grp * 2048, AUX_SC1);
        g1 = __builtin_amdgcn_raw_buffer_load_b128(rws, lane * 16 + 1024, W_Q + grp * 2048, AUX_SC1);
      } while (__any(((g0[1] ^ tg) | (g0[3] ^ tg) | (g1[1] ^ tg) | (g1[3] ^ tg)) != 0u) && --budget > 0);
      *(LAS u32x2*)(L + L_QB + lane * 8) = u32x2{g0[0], g0[2]};
      *(LAS u32x2*)(L + L_QB + 512 + lane * 8) = u32x2{g1[0], g1[2]};
      WAITL();
      ctl_wr(L, C_Q, 1);
      STAMP(6);
    } else {
      ctl_wait(L, C_Q, 1);
    }
#pragma unroll
    for (int s = 0; s < 8; ++s) qf[s] = *(LAS u32x4*)(L + L_QB + (lane & 1) * 512 + (s + 16 * (g4 & 1) + 8 * (g4 >> 1)) * 16);
#pragma unroll
    for (int i = 0; i < RPRE; ++i) score(kp[i], i);
  }
  for (int i = RPRE; i < NU / 4; ++i) {
    const int u = 4 * i + wave;
    landed_wait(L, seen, u + 1);
    u32x4 kf[8];
#pragma unroll
    for (int s = 0; s < 8; ++s) kf[s] = *(LAS u32x4*)(L + (u & (NS - 1)) * SLOT + ka + 16 * s);
    WAITL();
    ctl_wr(L, C_PROG + wave, min(u + 4, NU));
    score(kf, i);
  }

  mx = xrow_max(mx);
  if (lane < 2) *(LAS float*)(L + L_RED + wave * 8 + lane * 4) = mx;
  ctl_add(L, C_MX, lane);
  ctl_wait(L, C_MX, 4);
  {
    const LAS float* r = (const LAS float*)(L + L_RED + (lane & 1) * 4);
    const float M = fmaxf(fmaxf(r[0], r[2]), fmaxf(r[4], r[6]));
    LAS u32x4* sp = (LAS u32x4*)(L + L_STG + wave * 2048 + lane * 32);
    float ls = 0.f;
#pragma unroll
    for (int hh = 0; hh < 2; ++hh) {
      u32x4 v = sp[hh];
#pragma unroll
      for (int i = 0; i < 4; ++i) {
        const float p0 = __builtin_amdgcn_exp2f((bf_lo(v[i]) - M) * C2), p1 = __builtin_amdgcn_exp2f((bf_hi(v[i]) - M) * C2);
        ls += p0 + p1; v[i] = pack2(p0, p1);
      }
      sp[hh] = v;
    }
    ls += dpp<XOR2>(ls); ls += dpp<ROR4>(ls); ls += dpp<ROR8>(ls); ls = xrow_sum(ls);
    if (lane < 2) *(LAS float*)(L + L_RED + 32 + wave * 8 + lane * 4) = ls;
    ctl_add(L, C_P, lane);
    ctl_wait(L, C_P, 4);
    if (wave == 0) {
      if (lane < 2) {
        const float Ls = (r[8] + r[10]) + (r[12] + r[14]);
        __builtin_amdgcn_raw_buffer_store_b64(u32x2{pub(M), pub(Ls)}, rws, lane * 8, W_ML + row * 16, AUX_SC1);
      }
      STAMP(15);
    }
  }

  f32x4 acc[4];
#pragma unroll
  for (int c = 0; c < 4; ++c) acc[c] = f32x4{0.f, 0.f, 0.f, 0.f};
  {
    const int va = (2 * g4 + (q4 >> 1)) * VBLK + (q4 & 1) * 512 + (p4 >> 1) * 16 + (p4 & 1) * 8 + ((wave & 1) ? -128 : 128) * (q4 & 1) + 128 * wave;
    LAS char* p_rd = L + L_STG + (lane & 1) * 32 + g4 * 8;
    for (int k = 0; k < NU; ++k) {
      const u32x2 pb = *(LAS u32x2*)(p_rd + (k & 3) * 2048 + (k >> 2) * 64);
      landed_wait(L, seen, NU + k + 1);
      LAS char* sl = L + (k & (NS - 1)) * SLOT + va;
      u32x2 vf[4];
#pragma unroll
      for (int c = 0; c < 4; ++c) vf[c] = trread(sl + 32 * c);
      WAITL();
      ctl_wr(L, C_PROG + wave, NU + k + 1);
#pragma unroll
      for (int c = 0; c < 4; ++c) acc[c] = mfma16(vf[c], pb, acc[c]);
      if (k == 8 && wave == 0 && (is_wo || is_ffn || bid == 0)) {
        for (int s = 0; s < SPIN_CAP; ++s) {
          const u32x4 a = __builtin_amdgcn_raw_buffer_load_b128(rws, lane * 16, W_FLAG, AUX_SC1);
          const u32x4 c = __builtin_amdgcn_raw_buffer_load_b128(rws, lane * 16 + 1024, W_FLAG, AUX_SC1);
          const unsigned lo = ep[0] + 1u, hi = ep[1];
          if (__all(a[0] == lo && a[1] == hi && a[2] == lo && a[3] == hi && c[0] == lo && c[1] == hi && c[2] == lo && c[3] == hi)) break;
          __builtin_amdgcn_s_sleep(1);
          CFENCE();
        }
        ctl_wr(L, C_VER, 1);
        if (bid == 0 && lane == 0) __builtin_amdgcn_raw_buffer_store_b64(u32x2{ep[0] + 1u, ep[1]}, rws, 0, W_EPOCH, AUX_SC1);
      }
    }
  }
  if (wave == 3) STAMP(7);
  {
    const f32x4 a = (n16 & 2) ? acc[1] : acc[0], c = (n16 & 2) ? acc[3] : acc[2], o = (n16 & 4) ? c : a;
    if (n16 < 8)
      __builtin_amdgcn_raw_buffer_store_b128(u32x4{pub(o[0]), pub(o[1]), pub(o[2]), pub(o[3])}, rws,
                                             wave * 512 + (n16 & 1) * 256 + (n16 >> 1) * 64 + g4 * 16, W_PART + row * 2048, AUX_SC1);
  }
  if (wave == 3) STAMP(8);

  if (is_wo) {
    const int hf = lane >> 5, gsel = hf * 8 + x8;
    ctl_wait(L, C_VER, 1);
    u32x2 ml;
    for (int s = 0; s < SPIN_CAP; ++s) {
      ml = __builtin_amdgcn_raw_buffer_load_b64(rws, gsel * 256 + n16 * 16 + (g4 & 1) * 8, W_ML, AUX_SC1);
      if (__all(max(ml[0], ml[1]) != SENT)) break;
      __builtin_amdgcn_s_sleep(4);
      CFENCE();
    }
    const float mj = __uint_as_float(ml[0]);
    float wt = __builtin_amdgcn_exp2f((mj - row_max(mj)) * C2);
    wt *= 1.f / row_sum(wt * __uint_as_float(ml[1]));
    landed_wait(L, seen, 2 * NU + 1);
    u32x4 wf[16];
#pragma unroll
    for (int s = 0; s < 16; ++s) wf[s] = trread2(L + (32 * s + 8 * g4 + q4) * 128 + wave * 32 + 8 * p4, 512);
    if (wave == 0) STAMP(16);
    for (int s = 0; s < SPIN_CAP; ++s) {
      const unsigned probe = __builtin_amdgcn_raw_buffer_load_b32(rws, gsel * 32768 + n16 * 2048 + wave * 512 + (g4 & 1) * 256 + 252, W_PART, AUX_SC1);
      if (__builtin_popcountll(__ballot(probe == SENT)) <= 16) break;
      __builtin_amdgcn_s_sleep(2);
      CFENCE();
    }
    float wb[16];
#define BC_(k) wb[k] = dpp<NEWBC + k>(wt);
    BC_(0) BC_(1) BC_(2) BC_(3) BC_(4) BC_(5) BC_(6) BC_(7) BC_(8) BC_(9) BC_(10) BC_(11) BC_(12) BC_(13) BC_(14) BC_(15)
#undef BC_
    LAS char* ob_wr = pin(L, L_OB + hf * 1024 + (g4 & 1) * 512 + wave * 128 + n16 * 8);
    LAS char* ob_rd = pin(L, L_OB + (lane & 1) * 1024 + g4 * 16);
    int y_off = g4 * 16384 + wave * 64 + n16 * 4;
    asm volatile("" : "+v"(y_off));
    u32x4 pv[16];
#pragma unroll
    for (int k = 0; k < 16; ++k) pv[k] = u32x4{0u, 0u, 0u, SENT};
    f32x4 o0, o1;
    const int pvo = gsel * 32768 + wave * 512 + (lane & 31) * 16;
    int budget = SPIN_CAP;
    bool again;
    do {
      do {
#pragma unroll
        for (int k = 0; k < 16; k += 4) reload4(pv[k], pv[k + 1], pv[k + 2], pv[k + 3], pvo, srd, W_PART + k * 2048, W_PART + k * 2048 + 4096);
        LANDED8(pv, 0); LANDED8(pv, 8);
      } while (pending16(pv) && --budget > 0);
      o0 = wb[0] * __builtin_bit_cast(f32x4, pv[0]); o1 = wb[1] * __builtin_bit_cast(f32x4, pv[1]);
#pragma unroll
      for (int k = 2; k < 16; k += 2) { o0 += wb[k] * __builtin_bit_cast(f32x4, pv[k]); o1 += wb[k + 1] * __builtin_bit_cast(f32x4, pv[k + 1]); }
      o0 += o1;
      const float chk = (o0[0] + o0[1]) + (o0[2] + o0[3]);
      again = false;
      if (__builtin_expect(__any(chk != chk), 0)) again = rearm16(pv) && --budget > 0;
    } while (__builtin_expect(again, 0));
    if (wave == 0) STAMP(9);
    *(LAS u32x2*)ob_wr = u32x2{pack2(o0[0], o0[1]), pack2(o0[2], o0[3])};
    BARRIER();
    f32x4 d0 = {0.f, 0.f, 0.f, 0.f}, d1 = d0;
    u32x4 af[16];
#pragma unroll
    for (int s = 0; s < 16; ++s) af[s] = *(LAS u32x4*)(ob_rd + s * 64);
    __builtin_amdgcn_sched_barrier(0);
#pragma unroll
    for (int s = 0; s < 16; s += 2) { d0 = mfma32(af[s], wf[s], d0); d1 = mfma32(af[s + 1], wf[s + 1], d1); }
    if (lane < 32)
      __builtin_amdgcn_raw_buffer_store_b32(pub((g4 & 1) ? d0[1] + d1[1] : d0[0] + d1[0]), rws, y_off, W_Y + x8 * 2048 + rk * 256, AUX_SC1);
    if (wave == 0) STAMP(10);
  }
  if (!is_ffn) { FLUSH(); return; }

  const int ln = lane < 56 ? lane : 55;
  u32x2 xa = {0u, 0u}, xb = {0u, 0u};
  if (wave < 2) { xa = *(const u32x2*)(x + wave * DM + ln * 4); xb = *(const u32x2*)(x + wave * DM + 224 + ln * 4); }
  ctl_wait(L, C_VER, 1);
  landed_wait(L, seen, 2 * NU + 1);
  u32x4 gf[14], df[7];
#pragma unroll
  for (int s = 0; s < 14; ++s) gf[s] = trread2(L + (p4 >> 1) * 28672 + (32 * s + 8 * g4 + q4) * 64 + wave * 16 + 8 * (p4 & 1), 256);
#pragma unroll
  for (int i = 0; i < 7; ++i) df[i] = trread2(L + 57344 + (8 * g4 + q4) * 896 + (7 * wave + i) * 32 + 8 * p4, 4 * 896);
  LAS char* xn_rd = pin(L, L_XN + (lane & 1) * 896 + g4 * 16);
  LAS char* xn_wr = pin(L, L_XN + wave * 896 + lane * 8);
  LAS char* hb_wr = pin(L, L_HB + wave * 1792 + lane * 16);
  LAS char* gb_wr = pin(L, L_GB + g4 * 64 + (8 * wave + n16) * 2);
  LAS char* gb_rd = pin(L, L_GB + (lane & 1) * 64 + g4 * 16);
  int z_off = (((g4 & 1) * 28 + 7 * wave + (g4 >> 1)) * NFFN + f) * 64 + n16 * 4;
  asm volatile("" : "+v"(z_off));
  if (wave == 0) STAMP(17);
  if (wave < 2) {
    for (int s = 0; s < SPIN_CAP; ++s) {
      const unsigned probe = __builtin_amdgcn_raw_buffer_load_b32(rws, (ln / 7) * 2048 + (ln % 7) * 256 + 252, W_Y + wave * 16384, AUX_SC1);
      if (__builtin_popcountll(__ballot(probe == SENT)) <= 21) break;
      __builtin_amdgcn_s_sleep(2);
      CFENCE();
    }
    u32x4 yv[16];
#pragma unroll
    for (int k = 0; k < 16; ++k) yv[k] = u32x4{0u, 0u, 0u, SENT};
    f32x4 sa, sb;
    int budget = SPIN_CAP;
    bool again;
    do {
      do {
#pragma unroll
        for (int k = 0; k < 8; k += 4) {
          reload4(yv[k], yv[k + 1], yv[k + 2], yv[k + 3], ln * 16, srd, W_Y + wave * 16384 + k * 2048, W_Y + wave * 16384 + k * 2048 + 4096);
          reload4(yv[8 + k], yv[9 + k], yv[10 + k], yv[11 + k], ln * 16 + 896, srd, W_Y + wave * 16384 + k * 2048, W_Y + wave * 16384 + k * 2048 + 4096);
        }
        LANDED8(yv, 0); LANDED8(yv, 8);
      } while (pending16(yv) && --budget > 0);
      sa = __builtin_bit_cast(f32x4, yv[0]); sb = __builtin_bit_cast(f32x4, yv[8]);
#pragma unroll
      for (int k = 1; k < 8; ++k) { sa += __builtin_bit_cast(f32x4, yv[k]); sb += __builtin_bit_cast(f32x4, yv[8 + k]); }
      const f32x4 t = sa + sb;
      const float chk = (t[0] + t[1]) + (t[2] + t[3]);
      again = false;
      if (__builtin_expect(__any(chk != chk), 0)) again = rearm16(yv) && --budget > 0;
    } while (__builtin_expect(again, 0));
    if (wave == 0) STAMP(11);
    float v[8];
#pragma unroll
    for (int i = 0; i < 2; ++i) {
      const unsigned ra = pack2(sa[2 * i], sa[2 * i + 1]), rb = pack2(sb[2 * i], sb[2 * i + 1]);
      const unsigned ha = pack2(bf_lo(xa[i]) + bf_lo(ra), bf_hi(xa[i]) + bf_hi(ra)), hb = pack2(bf_lo(xb[i]) + bf_lo(rb), bf_hi(xb[i]) + bf_hi(rb));
      v[2 * i] = bf_lo(ha); v[2 * i + 1] = bf_hi(ha); v[4 + 2 * i] = bf_lo(hb); v[5 + 2 * i] = bf_hi(hb);
    }
    const u32x4 o = layernorm8(v, lane < 56);
    if (lane < 56) {
      *(LAS u32x2*)xn_wr = u32x2{o[0], o[1]};
      *(LAS u32x2*)(xn_wr + 448) = u32x2{o[2], o[3]};
      *(LAS f32x4*)hb_wr = f32x4{v[0], v[1], v[2], v[3]};
      *(LAS f32x4*)(hb_wr + 896) = f32x4{v[4], v[5], v[6], v[7]};
    }
  }
  BARRIER();
  {
    f32x4 d0 = {0.f, 0.f, 0.f, 0.f}, d1 = d0;
    u32x4 af[14];
#pragma unroll
    for (int s = 0; s < 14; ++s) af[s] = *(LAS u32x4*)(xn_rd + s * 64);
    __builtin_amdgcn_sched_barrier(0);
#pragma unroll
    for (int s = 0; s < 14; s += 2) { d0 = mfma32(af[s], gf[s], d0); d1 = mfma32(af[s + 1], gf[s + 1], d1); }
    const float a = rbf((g4 & 1) ? d0[1] + d1[1] : d0[0] + d1[0]), u = dpp<ROR8>(a);
    const float z = 0.7978845608028654f * (a + 0.044715f * a * a * a);
    const float ge = rbf(a * __builtin_amdgcn_rcpf(1.f + __builtin_amdgcn_exp2f(-2.f * LOG2E * z)));
    if (lane < 32 && n16 < 8) *(LAS u16*)gb_wr = (u16)pack2(ge * u, 0.f);
  }
  BARRIER();
  {
    const u32x4 a = *(LAS u32x4*)gb_rd;
    f32x4 d[8];
#pragma unroll
    for (int i = 0; i < 7; ++i) d[i] = mfma32(a, df[i], f32x4{0.f, 0.f, 0.f, 0.f});
    d[7] = d[6];
#pragma unroll
    for (int i = 0; i < 4; ++i) {
      const float v0 = (g4 & 1) ? d[2 * i][1] : d[2 * i][0], v1 = (g4 & 1) ? d[2 * i + 1][1] : d[2 * i + 1][0];
      if (i < 3 || lane < 32) __builtin_amdgcn_raw_buffer_store_b32(pub((g4 >> 1) ? v1 : v0), rws, z_off, W_Z + 2 * i * NFFN * 64, AUX_SC1);
    }
  }
  if (wave != 0) { FLUSH(); return; }
  STAMP(12);

  {
    const int ob = f / 28, oc = (f % 28) * 16 + p4 * 4;
    const f32x4 hv = *(LAS f32x4*)(L + L_HB + ob * 1792 + oc * 4);
    u32x4 zv[4];
    int budget = SPIN_CAP;
    unsigned mxx;
    do {
      CFENCE();
#pragma unroll
      for (int k = 0; k < 4; ++k) zv[k] = __builtin_amdgcn_raw_buffer_load_b128(rws, (k < 3 ? lane : (lane & 31)) * 16 + k * 1024, W_Z + f * (NFFN * 64), AUX_SC1);
      mxx = max(max(umax4(zv[0]), umax4(zv[1])), max(umax4(zv[2]), umax4(zv[3])));
    } while (__any(mxx == SENT) && --budget > 0);
    STAMP(13);
    f32x4 sum = (__builtin_bit_cast(f32x4, zv[0]) + __builtin_bit_cast(f32x4, zv[1])) + __builtin_bit_cast(f32x4, zv[2]);
    if (lane < 32) sum += __builtin_bit_cast(f32x4, zv[3]);
    float r[4];
#pragma unroll
    for (int i = 0; i < 4; ++i) { float t = sum[i]; t += dpp<ROR4>(t); t += dpp<ROR8>(t); r[i] = hv[i] + rbf(xrow_sum(t)); }
    if (lane < 4) *(u32x2*)(out + ob * DM + oc) = u32x2{pack2(r[0], r[1]), pack2(r[2], r[3])};
    STAMP(14); FLUSH();
  }
}

extern "C" void kernel_launch(void* const* d_in, const int* in_sizes, int n_in, void* d_out, int out_size, void* d_ws, size_t ws_size,
                              hipStream_t stream) {
  fused_decode<<<256, 320, 0, stream>>>((const u16*)d_in[0], (const u16*)d_in[1], (const u16*)d_in[2], (const u16*)d_in[3], (const u16*)d_in[4],
                                        (const u16*)d_in[6], (const u16*)d_in[7], (const u16*)d_in[8], (u16*)d_out, (char*)d_ws);
}
```
